```python
import torch

def _softmax(x, axis=-1):
    return torch.softmax(x, dim=axis)
def _gelu(x):
    return torch.nn.functional.gelu(x, approximate="tanh")
def _silu(x):
    return torch.nn.functional.silu(x)
def _sigmoid(x):
    return torch.sigmoid(x)
def _layernorm(x, axis=-1, eps=1e-5):
    m = x.mean(dim=axis, keepdim=True)
    v = x.var(dim=axis, keepdim=True, unbiased=False)
    return (x - m) / torch.sqrt(v + eps)
def _scatter_add(tgt, idx, src, axis=0):
    return tgt.clone().index_add_(axis, idx, src)
def _segment_sum(src, seg, n):
    out = torch.zeros((n,) + src.shape[1:], dtype=src.dtype)
    return out.index_add_(0, seg, src)
def _pad(x, pw):
    flat = tuple(v for pair in reversed(pw) for v in pair)
    return torch.nn.functional.pad(x, flat)

kernel_name = 'gated_mix__mha_block_cached__glu_gated_swiglu__decode'

def setup_inputs():

    _w = (lambda a: (lambda t: t.to(torch.bfloat16) if t.is_floating_point() else t)(torch.as_tensor(a)))
    def _A_setup():
        B, Sq, D = 4, 1, 512
        H, Sk, Dh = 32, 8192, 128
        return {
            "x": _w((lambda *s: torch.randn(*s, dtype=torch.bfloat16) * 0.05)(B, Sq, D)),
            "k_cache": _w((lambda *s: torch.randn(*s, dtype=torch.bfloat16) * 6.4)(B, H, Sk, Dh)),
            "v_cache": _w((lambda *s: torch.randn(*s, dtype=torch.bfloat16) * 0.2)(B, H, Sk, Dh)),
            "wq": _w((lambda *s: torch.randn(*s, dtype=torch.bfloat16) * 6.4)(D, H * Dh) * (D ** -0.5)),
            "wo": _w((lambda *s: torch.randn(*s, dtype=torch.bfloat16) * 0.2)(H * Dh, D) * ((H * Dh) ** -0.5)),
        }
    _A_in = _A_setup()

    def _B_setup():
        B, S, D, F = 4, 1, 512, 1792
        return {
            "x": _w((lambda *s: torch.randn(*s, dtype=torch.bfloat16) * 0.05)(B, S, D)),
            "w1": _w((lambda *s: torch.randn(*s, dtype=torch.bfloat16) * 0.8)(D, F) * (2.0 / D) ** 0.5),
            "w3": _w((lambda *s: torch.randn(*s, dtype=torch.bfloat16) * 0.8)(D, F) * (2.0 / D) ** 0.5),
            "w2": _w((lambda *s: torch.randn(*s, dtype=torch.bfloat16) * 0.8)(F, D) * (2.0 / F) ** 0.5),
        }
    _B_in = _B_setup()

    return {
        "A_x": _A_in["x"],
        "A_k_cache": _A_in["k_cache"],
        "A_v_cache": _A_in["v_cache"],
        "A_wq": _A_in["wq"],
        "A_wo": _A_in["wo"],
        "B_x": _B_in["x"],
        "B_w1": _B_in["w1"],
        "B_w3": _B_in["w3"],
        "B_w2": _B_in["w2"],
        "gate_w": _w((lambda *s: torch.randn(*s, dtype=torch.bfloat16) * 0.05)(512) * 16.0),
    }

def reference(A_x, A_k_cache, A_v_cache, A_wq, A_wo, B_x, B_w1, B_w3, B_w2, gate_w):
    def _A_ref(x, k_cache, v_cache, wq, wo):
        B, Sq, D = x.shape
        H, Dh = 32, 128
        q = (lambda x, *d: x.permute(*d))((x @ wq).reshape(B, Sq, H, Dh), 0, 2, 1, 3)
        scores = torch.einsum("bhqd,bhkd->bhqk", q, k_cache) * (Dh ** -0.5)
        p = _softmax(scores, axis=-1)
        o = torch.einsum("bhqk,bhkd->bhqd", p, v_cache)
        return (lambda x, *d: x.permute(*d))(o, 0, 2, 1, 3).reshape(B, Sq, H * Dh) @ wo

    def _B_ref(x, w1, w3, w2):
        gate = torch.einsum("bsd,df->bsf", x, w1)
        up = torch.einsum("bsd,df->bsf", x, w3)
        h = _silu(gate) * up
        out = torch.einsum("bsf,fd->bsd", h, w2)
        return out

    x = A_x
    g = _sigmoid((x * gate_w).sum(-1, keepdims=True))
    a = _A_ref(x, A_k_cache, A_v_cache, A_wq, A_wo)
    b = _B_ref(x, B_w1, B_w3, B_w2)
    return g * a + (1 - g) * b
```

```cpp
#include <hip/hip_runtime.h>
#include <hip/hip_bf16.h>
#include <stdint.h>

typedef unsigned u32x4 __attribute__((ext_vector_type(4)));
typedef float f32x4 __attribute__((ext_vector_type(4)));
typedef float f32x2 __attribute__((ext_vector_type(2)));
typedef __bf16 bf16x2 __attribute__((ext_vector_type(2)));
typedef unsigned long long u64;
#define GAS __attribute__((address_space(1)))
#define RLX_AGENT __ATOMIC_RELAXED, __HIP_MEMORY_SCOPE_AGENT
#define RLX_WG __ATOMIC_RELAXED, __HIP_MEMORY_SCOPE_WORKGROUP

#define DEPTH 8
#define WNT 1

constexpr int NB = 4, NH = 32, SK = 8192, DH = 128, DM = 512, DF = 1792;
constexpr int NWG = 256, NTHR = 512;
constexpr unsigned SPIN_CAP = 1u << 18;
constexpr size_t WS_EPOCH = 0;
constexpr size_t WS_LSE = 4096;
constexpr size_t WS_Y = 1u << 20;
constexpr size_t WS_STAMP = 4u << 20;

constexpr int L_SC = 0;
constexpr int L_QP = L_SC + 4 * 16 * 64;
constexpr int L_OP = L_QP + 4 * 128;
constexpr int L_FP = L_OP + 4 * 128;
constexpr int L_ML = L_FP + 4 * 64;
constexpr int L_CW = L_ML + 8;
constexpr int L_CNT = L_CW + 4;
constexpr int L_END = L_CNT + 4;

__device__ __forceinline__ u32x4 ld_nt(const void* p) { return __builtin_nontemporal_load((const u32x4*)p); }
__device__ __forceinline__ u32x4 ld_w(const void* p) {
  return __builtin_nontemporal_load((const u32x4*)p);
}
__device__ __forceinline__ float dot2(unsigned a, unsigned b, float acc) {
  return __builtin_amdgcn_fdot2_f32_bf16(__builtin_bit_cast(bf16x2, a), __builtin_bit_cast(bf16x2, b), acc, false);
}
__device__ __forceinline__ float bf_lo(unsigned v) { return __builtin_bit_cast(float, v << 16); }
__device__ __forceinline__ float bf_hi(unsigned v) { return __builtin_bit_cast(float, v & 0xffff0000u); }
__device__ __forceinline__ unsigned pack_bf16(float lo, float hi) {
  bf16x2 r = {(__bf16)lo, (__bf16)hi};
  return __builtin_bit_cast(unsigned, r);
}
template <int CTRL> __device__ __forceinline__ float dpp(float x) {
  return __builtin_bit_cast(float, __builtin_amdgcn_mov_dpp(__builtin_bit_cast(int, x), CTRL, 0xf, 0xf, true));
}
template <int CTRL> __device__ __forceinline__ unsigned dppu(unsigned x) {
  return (unsigned)__builtin_amdgcn_mov_dpp((int)x, CTRL, 0xf, 0xf, true);
}
constexpr int XOR1 = 0xB1, XOR2 = 0x4E, XOR7 = 0x141, ROR8 = 0x128, BCAST = 0x150;
__device__ __forceinline__ float rs32(float a, float b) {
  auto s = __builtin_amdgcn_permlane32_swap(__float_as_uint(a), __float_as_uint(b), false, false);
  return __uint_as_float(s[0]) + __uint_as_float(s[1]);
}
__device__ __forceinline__ float rs16(float a, float b) {
  auto s = __builtin_amdgcn_permlane16_swap(__float_as_uint(a), __float_as_uint(b), false, false);
  return __uint_as_float(s[0]) + __uint_as_float(s[1]);
}
__device__ __forceinline__ float xrow_max(float x) {
  auto s = __builtin_amdgcn_permlane16_swap(__float_as_uint(x), __float_as_uint(x), false, false);
  x = fmaxf(__uint_as_float(s[0]), __uint_as_float(s[1]));
  auto t = __builtin_amdgcn_permlane32_swap(__float_as_uint(x), __float_as_uint(x), false, false);
  return fmaxf(__uint_as_float(t[0]), __uint_as_float(t[1]));
}
__device__ __forceinline__ float wave_max(float x) {
  x = fmaxf(x, dpp<XOR1>(x)); x = fmaxf(x, dpp<XOR2>(x)); x = fmaxf(x, dpp<XOR7>(x)); x = fmaxf(x, dpp<ROR8>(x));
  return xrow_max(x);
}
__device__ __forceinline__ float wave_sum(float x) {
  x += dpp<XOR1>(x); x += dpp<XOR2>(x); x += dpp<XOR7>(x); x += dpp<ROR8>(x);
  x = rs16(x, x);
  return rs32(x, x);
}
__device__ __forceinline__ f32x2 row_reduce_scatter(const float (&acc)[8]) {
  float u0 = rs32(acc[0], acc[4]), u1 = rs32(acc[1], acc[5]), u2 = rs32(acc[2], acc[6]), u3 = rs32(acc[3], acc[7]);
  f32x2 z; z[0] = rs16(u0, u2); z[1] = rs16(u1, u3);
  return z;
}
__device__ __forceinline__ void fma8(float (&acc)[8], float p, const u32x4& v) {
#pragma unroll
  for (int k = 0; k < 4; ++k) { acc[2 * k] = fmaf(p, bf_lo(v[k]), acc[2 * k]); acc[2 * k + 1] = fmaf(p, bf_hi(v[k]), acc[2 * k + 1]); }
}
template <int T> __device__ __forceinline__ float bcast16(float x) { return dpp<BCAST + T>(x); }
#define LDS_FENCE() asm volatile("s_waitcnt lgkmcnt(0)" ::: "memory")
#define BARRIER() do { asm volatile("s_waitcnt lgkmcnt(0)" ::: "memory"); __builtin_amdgcn_s_barrier(); asm volatile("" ::: "memory"); } while (0)
#define SB() __builtin_amdgcn_sched_barrier(0)

#define STAMP(i) do {} while (0)

template <int I> struct IC { static constexpr int v = I; };
template <int N, int I = 0, class F> __device__ __forceinline__ void static_for(F&& f) {
  if constexpr (I < N) { f(IC<I>{}); static_for<N, I + 1>(f); }
}
__device__ __forceinline__ constexpr int toff(int t) { return (t >> 2) * 16384 + (t & 3) * 1024; }

template <bool FIRST>
__device__ __forceinline__ float k_group(u32x4 (&buf)[16], const u32x4& q, const char* cur, const char* nxt, int c) {
  float x[16];
  static_for<16>([&](auto tt) {
    constexpr int t = decltype(tt)::v;
    float d = dot2(buf[t][0], q[0], 0.f);
    d = dot2(buf[t][1], q[1], d); d = dot2(buf[t][2], q[2], d); d = dot2(buf[t][3], q[3], d);
    x[t] = d;
    SB();
    if constexpr (t + DEPTH < 16) { if constexpr (!FIRST) buf[t + DEPTH] = ld_nt(cur + toff(t + DEPTH)); }
    else buf[t + DEPTH - 16] = ld_nt(nxt + toff(t + DEPTH - 16));
    SB();
  });
#define TR_STEP(HALF, CTRL, BIT) _Pragma("unroll") for (int j = 0; j < (HALF); ++j) { \
    const float keep = (c & (BIT)) ? x[j + (HALF)] : x[j], send = (c & (BIT)) ? x[j] : x[j + (HALF)]; x[j] = keep + dpp<CTRL>(send); }
  TR_STEP(8, ROR8, 8) TR_STEP(4, XOR7, 4) TR_STEP(2, XOR2, 2) TR_STEP(1, XOR1, 1)
#undef TR_STEP
  return x[0];
}
template <bool LAST>
__device__ __forceinline__ void v_group(u32x4 (&buf)[16], float (&acc)[8], float p, const char* cur, const char* nxt) {
  static_for<16>([&](auto tt) {
    constexpr int t = decltype(tt)::v;
    fma8(acc, bcast16<t>(p), buf[t]);
    SB();
    if constexpr (t + DEPTH < 16) buf[t + DEPTH] = ld_nt(cur + toff(t + DEPTH));
    else if constexpr (!LAST) buf[t + DEPTH - 16] = ld_nt(nxt + toff(t + DEPTH - 16));
    SB();
  });
}

__global__ void __launch_bounds__(NTHR, 2)
fused_decode_kernel(const unsigned short* __restrict__ x, const char* __restrict__ kc, const char* __restrict__ vc,
                    const char* __restrict__ wq, const char* __restrict__ wo, const char* __restrict__ w1,
                    const char* __restrict__ w3, const char* __restrict__ w2, const unsigned short* __restrict__ gw,
                    unsigned short* __restrict__ out, char* __restrict__ ws) {
  __shared__ __attribute__((aligned(16))) float lds[L_END];
  unsigned* ldsu = (unsigned*)lds;
  const int tid = threadIdx.x, lane = tid & 63, wave = __builtin_amdgcn_readfirstlane(tid >> 6);
  const int r = lane >> 4, c = lane & 15;
  const int bid = blockIdx.x, idx = bid >> 3;
  const int h = (bid & 7) * 4 + (idx & 3), b = idx >> 3, s = (idx >> 2) & 1;
  const int p = h * 2 + s;
  STAMP(0);

  if (wave < 4) {
    const size_t base = ((size_t)(b * NH + h) * SK + (size_t)s * (SK / 2)) * (DH * 2) + wave * 4096 + lane * 16;
    const char* kp = kc + base;
    const char* vp = vc + base;
    u32x4 buf[16];
    static_for<16>([&](auto tt) { constexpr int t = decltype(tt)::v; buf[t] = ld_nt(kp + toff(t)); SB(); });
    BARRIER();
    STAMP(1);
    u32x4 q;
    {
      f32x4 a0 = *(const f32x4*)(lds + L_QP + 8 * c), a1 = *(const f32x4*)(lds + L_QP + 8 * c + 4);
#pragma unroll
      for (int w = 1; w < 4; ++w) { a0 += *(const f32x4*)(lds + L_QP + w * 128 + 8 * c); a1 += *(const f32x4*)(lds + L_QP + w * 128 + 8 * c + 4); }
      q[0] = pack_bf16(a0[0], a0[1]); q[1] = pack_bf16(a0[2], a0[3]); q[2] = pack_bf16(a1[0], a1[1]); q[3] = pack_bf16(a1[2], a1[3]);
    }
    constexpr float C2 = 0.088388347648318440f * 1.4426950408889634f;
    float* sc = lds + L_SC + wave * 1024 + lane;
    float mx = k_group<true>(buf, q, kp, kp + 65536, c) * C2;
    sc[0] = mx;
    STAMP(2);
#pragma unroll 1
    for (int g = 1; g < 16; ++g) {
      const char* cur = kp + g * 65536;
      const char* nxt = (g < 15) ? cur + 65536 : vp;
      const float sv = k_group<false>(buf, q, cur, nxt, c) * C2;
      sc[g * 64] = sv;
      mx = fmaxf(mx, sv);
    }
    STAMP(3);
    mx = wave_max(mx);
    float l = 0.f;
#pragma unroll
    for (int g = 0; g < 16; ++g) { const float pv = __builtin_amdgcn_exp2f(sc[g * 64] - mx); sc[g * 64] = pv; l += pv; }
    l = wave_sum(l);
    if (lane == 0) {
      lds[L_ML + wave] = mx; lds[L_ML + 4 + wave] = l;
      LDS_FENCE();
      __hip_atomic_fetch_add(ldsu + L_CNT + 0, 1u, RLX_WG);
    }
    float acc[8];
#pragma unroll
    for (int k = 0; k < 8; ++k) acc[k] = 0.f;
    float pc = sc[0];
#pragma unroll 1
    for (int g = 0; g < 15; ++g) {
      const char* cur = vp + g * 65536;
      const float pn = sc[(g + 1) * 64];
      v_group<false>(buf, acc, pc, cur, cur + 65536);
      pc = pn;
    }
    v_group<true>(buf, acc, pc, vp + 15 * 65536, vp);
    STAMP(4);
    *(f32x2*)(lds + L_OP + wave * 128 + 8 * c + 2 * r) = row_reduce_scatter(acc);
    BARRIER();
    STAMP(5);
    if (wave == 0) {
      const unsigned e_lo = ldsu[L_CNT + 2], e_hi = ldsu[L_CNT + 3];
      const u64 ep = (((u64)e_hi << 32) | e_lo) + 1;
      const unsigned t_hi = (unsigned)(ep >> 8), t_lo = (unsigned)ep & 0xffu;
      GAS u64* yin = (GAS u64*)(ws + WS_Y) + ((size_t)(b * 64 + p) * 64) * 8 + lane;
      float v[8]; unsigned pend = 0xffu;
      for (unsigned spins = 0; spins < SPIN_CAP; ++spins) {
        u64 gv[8];
#pragma unroll
        for (int i = 0; i < 8; ++i) gv[i] = __hip_atomic_load(yin + i * 64, RLX_AGENT);
#pragma unroll
        for (int i = 0; i < 8; ++i) {
          const unsigned lo = (unsigned)gv[i], hi = (unsigned)(gv[i] >> 32);
          if (hi == t_hi && (lo >> 24) == t_lo) { v[i] = __uint_as_float(lo << 8); pend &= ~(1u << i); }
        }
        if (__all(pend == 0u)) break;
        __builtin_amdgcn_s_sleep(1);
      }
      float sum = ((v[0] + v[1]) + (v[2] + v[3])) + ((v[4] + v[5]) + (v[6] + v[7]));
      if (!__all(pend == 0u)) sum = __uint_as_float(0x7fc00000u);
      sum += dpp<ROR8>(sum);
      sum = rs16(sum, sum);
      sum = rs32(sum, sum);
      if (lane < 8) {
        const int col = 128 * (p >> 4) + 8 * (8 * ((p >> 1) & 1) + lane) + 2 * ((p >> 2) & 3) + (p & 1);
        out[b * DM + col] = (unsigned short)pack_bf16(sum, 0.f);
      }
      if (p == 0 && lane == 0) __hip_atomic_store((GAS u64*)(ws + WS_EPOCH + b * 256), ep, RLX_AGENT);
      STAMP(6);
    }
  } else {
    const int hw = wave - 4;
    const unsigned short* xb = x + b * DM;
    u64 ep = 0;
    if (hw == 0) ep = __hip_atomic_load((GAS u64*)(ws + WS_EPOCH + b * 256), RLX_AGENT);
    {
      const float x0 = bf_lo(xb[128 * hw + 4 * c + r]), x1 = bf_lo(xb[128 * hw + 64 + 4 * c + r]);
      const char* wp = wq + ((size_t)(128 * hw + r) * (NH * DH) + h * DH + 8 * c) * 2;
      u32x4 wv[32];
      static_for<32>([&](auto ii) { constexpr int i = decltype(ii)::v; wv[i] = ld_w(wp + (size_t)i * 4 * NH * DH * 2); SB(); });
      float acc[8];
#pragma unroll
      for (int k = 0; k < 8; ++k) acc[k] = 0.f;
      static_for<32>([&](auto ii) { constexpr int i = decltype(ii)::v; fma8(acc, bcast16<i & 15>(i < 16 ? x0 : x1), wv[i]); SB(); });
      *(f32x2*)(lds + L_QP + hw * 128 + 8 * c + 2 * r) = row_reduce_scatter(acc);
    }
    if (hw == 0 && lane == 0) { ldsu[L_CNT + 0] = 0u; ldsu[L_CNT + 1] = 0u; ldsu[L_CNT + 2] = (unsigned)ep; ldsu[L_CNT + 3] = (unsigned)(ep >> 32); }
    BARRIER();
    STAMP(1);
    ep = (((u64)ldsu[L_CNT + 3] << 32) | ldsu[L_CNT + 2]) + 1;
    float g;
    {
      const u32x4 xv = *(const u32x4*)(xb + 8 * lane), gv = *(const u32x4*)(gw + 8 * lane);
      float z = dot2(xv[0], gv[0], 0.f); z = dot2(xv[1], gv[1], z); z = dot2(xv[2], gv[2], z); z = dot2(xv[3], gv[3], z);
      z = wave_sum(z);
      g = 1.f / (1.f + __expf(-z));
    }
    float yacc[8];
#pragma unroll
    for (int k = 0; k < 8; ++k) yacc[k] = 0.f;
    if (p < DF / 32) {
      const int r16 = lane >> 2, c4 = lane & 3;
      const size_t fo = ((size_t)(128 * hw + r16) * DF + 32 * p + 8 * c4) * 2;
      u32x4 a[8], u[8]; float xk[8];
#pragma unroll
      for (int i = 0; i < 8; ++i) { a[i] = ld_w(w1 + fo + (size_t)i * 16 * DF * 2); u[i] = ld_w(w3 + fo + (size_t)i * 16 * DF * 2); xk[i] = bf_lo(xb[128 * hw + 16 * i + r16]); }
      u32x4 dv[8];
#pragma unroll
      for (int i = 0; i < 8; ++i) dv[i] = ld_w(w2 + ((size_t)(32 * p + 8 * r + i) * DM + 128 * hw + 8 * c) * 2);
      float ga[8], ua[8];
#pragma unroll
      for (int k = 0; k < 8; ++k) { ga[k] = 0.f; ua[k] = 0.f; }
#pragma unroll
      for (int i = 0; i < 8; ++i) { fma8(ga, xk[i], a[i]); fma8(ua, xk[i], u[i]); }
#pragma unroll
      for (int k = 0; k < 8; ++k) {
#pragma unroll
        for (int m = 4; m < 64; m <<= 1) { ga[k] += __shfl_xor(ga[k], m); ua[k] += __shfl_xor(ua[k], m); }
      }
      if (lane < 4) {
#pragma unroll
        for (int k = 0; k < 8; ++k) { lds[L_FP + hw * 64 + 8 * lane + k] = ga[k]; lds[L_FP + hw * 64 + 32 + 8 * lane + k] = ua[k]; }
      }
      LDS_FENCE();
      if (lane == 0) __hip_atomic_fetch_add(ldsu + L_CNT + 1, 1u, RLX_WG);
      for (unsigned spins = 0; spins < (SPIN_CAP << 4); ++spins) {
        if (__hip_atomic_load(ldsu + L_CNT + 1, RLX_WG) >= 4u) break;
        __builtin_amdgcn_s_sleep(2);
      }
      asm volatile("" ::: "memory");
      const int f = 8 * r + (c & 7);
      const float gt = (lds[L_FP + f] + lds[L_FP + 64 + f]) + (lds[L_FP + 128 + f] + lds[L_FP + 192 + f]);
      const float up = (lds[L_FP + 32 + f] + lds[L_FP + 96 + f]) + (lds[L_FP + 160 + f] + lds[L_FP + 224 + f]);
      const float hv = (1.f - g) * (gt / (1.f + __expf(-gt))) * up;
      static_for<8>([&](auto ii) { constexpr int i = decltype(ii)::v; fma8(yacc, bcast16<i>(hv), dv[i]); });
    }
    STAMP(2);
    u32x4 wv[32];
    {
      const char* wp = wo + ((size_t)(h * DH + 32 * r) * DM + 128 * hw + 8 * c) * 2;
#pragma unroll
      for (int i = 0; i < 32; ++i) wv[i] = ld_w(wp + (size_t)i * DM * 2);
#pragma unroll
      for (int i = 0; i < 32; i += 2) {
#pragma unroll
        for (int k = 0; k < 4; ++k) {
          const unsigned e0 = wv[i][k], e1 = wv[i + 1][k];
          wv[i][k] = __builtin_amdgcn_perm(e1, e0, 0x05040100u);
          wv[i + 1][k] = __builtin_amdgcn_perm(e1, e0, 0x07060302u);
        }
        asm volatile("" : "+v"(wv[i]), "+v"(wv[i + 1]));
      }
    }
    STAMP(3);
    if (hw == 0) {
      for (unsigned spins = 0; spins < (SPIN_CAP << 4); ++spins) {
        if (__hip_atomic_load(ldsu + L_CNT + 0, RLX_WG) >= 4u) break;
        __builtin_amdgcn_s_sleep(16);
      }
      asm volatile("" ::: "memory");
      const f32x4 m4 = *(const f32x4*)(lds + L_ML), l4 = *(const f32x4*)(lds + L_ML + 4);
      const float mw = fmaxf(fmaxf(m4[0], m4[1]), fmaxf(m4[2], m4[3]));
      float lw = 0.f;
#pragma unroll
      for (int w = 0; w < 4; ++w) lw = fmaf(l4[w], __builtin_amdgcn_exp2f(m4[w] - mw), lw);
      const float lse = mw + __builtin_amdgcn_logf(lw);
      const unsigned tag = (unsigned)ep;
      GAS u64* lg = (GAS u64*)(ws + WS_LSE);
      if (lane == 0) __hip_atomic_store(lg + (size_t)(b * 64 + p) * 16, ((u64)tag << 32) | __float_as_uint(lse), RLX_AGENT);
      float lo2 = __uint_as_float(0x7fc00000u);
      for (unsigned spins = 0; spins < SPIN_CAP; ++spins) {
        const u64 o = __hip_atomic_load(lg + (size_t)(b * 64 + (p ^ 1)) * 16, RLX_AGENT);
        if ((unsigned)(o >> 32) == tag) { lo2 = __uint_as_float((unsigned)o); break; }
        __builtin_amdgcn_s_sleep(16);
      }
      const float mt = fmaxf(lse, lo2);
      const float lt = mt + __builtin_amdgcn_logf(__builtin_amdgcn_exp2f(lse - mt) + __builtin_amdgcn_exp2f(lo2 - mt));
      if (lane < 4) lds[L_CW + lane] = g * __builtin_amdgcn_exp2f(lds[L_ML + lane] - lt);
    }
    STAMP(4);
    BARRIER();
    STAMP(5);
    {
      const f32x4 cw = *(const f32x4*)(lds + L_CW);
      f32x2 o = cw[0] * *(const f32x2*)(lds + L_OP + 2 * lane);
#pragma unroll
      for (int w = 1; w < 4; ++w) o += cw[w] * *(const f32x2*)(lds + L_OP + w * 128 + 2 * lane);
      const unsigned op = pack_bf16(o[0], o[1]);
      static_for<16>([&](auto ii) {
        constexpr int i = decltype(ii)::v;
        const unsigned ob = dppu<BCAST + i>(op);
#pragma unroll
        for (int k = 0; k < 4; ++k) { yacc[2 * k] = dot2(wv[2 * i][k], ob, yacc[2 * k]); yacc[2 * k + 1] = dot2(wv[2 * i + 1][k], ob, yacc[2 * k + 1]); }
      });
      const f32x2 y = row_reduce_scatter(yacc);
      const unsigned t_hi = (unsigned)(ep >> 8), t_lo = ((unsigned)ep & 0xffu) << 24;
      GAS u64* yo = (GAS u64*)(ws + WS_Y) + ((size_t)(b * 64 + ((hw * 4 + r) * 2 + (c >> 3)) * 2) * 64 + p) * 8 + (c & 7);
      __hip_atomic_store(yo, ((u64)t_hi << 32) | (t_lo | ((__float_as_uint(y[0]) + 0x80u) >> 8)), RLX_AGENT);
      __hip_atomic_store(yo + 64 * 8, ((u64)t_hi << 32) | (t_lo | ((__float_as_uint(y[1]) + 0x80u) >> 8)), RLX_AGENT);
    }
    STAMP(6);
  }
}

extern "C" void kernel_launch(void* const* d_in, const int* in_sizes, int n_in, void* d_out, int out_size, void* d_ws,
                              size_t ws_size, hipStream_t stream) {
  (void)in_sizes; (void)n_in; (void)out_size; (void)ws_size;
  fused_decode_kernel<<<NWG, NTHR, 0, stream>>>((const unsigned short*)d_in[0], (const char*)d_in[1], (const char*)d_in[2],
                                                (const char*)d_in[3], (const char*)d_in[4], (const char*)d_in[6],
                                                (const char*)d_in[7], (const char*)d_in[8], (const unsigned short*)d_in[9],
                                                (unsigned short*)d_out, (char*)d_ws);
}
```

```cpp
#include <hip/hip_runtime.h>
#include <hip/hip_bf16.h>
#include <stdint.h>

typedef unsigned u32x2 __attribute__((ext_vector_type(2)));
typedef unsigned u32x4 __attribute__((ext_vector_type(4)));
typedef unsigned u32x8 __attribute__((ext_vector_type(8)));
typedef float f32x2 __attribute__((ext_vector_type(2)));
typedef float f32x4 __attribute__((ext_vector_type(4)));
typedef __bf16 bf16x2 __attribute__((ext_vector_type(2)));
typedef __bf16 bf16x8 __attribute__((ext_vector_type(8)));
typedef unsigned long long u64;
#define GAS __attribute__((address_space(1)))
#define RLX_AGENT __ATOMIC_RELAXED, __HIP_MEMORY_SCOPE_AGENT
#define RLX_WG __ATOMIC_RELAXED, __HIP_MEMORY_SCOPE_WORKGROUP

constexpr int NH = 32, DH = 128, DM = 512, DF = 1792;
constexpr int NWG = 256, NTHR = 512;
constexpr unsigned SPIN_CAP = 1u << 18;
constexpr float LOG2E = 1.4426950408889634f;

#define TSTREAM 69.0
#define TDELTA 1.65
#define TEXTRA 0.6
#define YSCHED 1.78
constexpr double YWAIT = 0.55;
constexpr double QWAIT = 4.0;
constexpr double TREAD = 0.85;
constexpr double PF_A = 0.1, PF_B = 0.6, PF_C = 0.8;
constexpr double HSP_F = 2.4, HSP_O = 1.2, HSTAG = 0.33;
constexpr int LAG = 3;
constexpr int NPRE = 2;
constexpr unsigned PH = 27, YEARLY = 19;
constexpr u64 XM = 0x0c281a191a0b0036ull;
constexpr int CODE_KIB = 23;
constexpr unsigned TAU = (unsigned)((TSTREAM) * 200.0);
constexpr unsigned steps(double us) { return (unsigned)(us * 100.0); }

constexpr size_t WS_EPOCH = 0;
constexpr size_t WS_T0 = 1024;
constexpr size_t WS_ML = 4096;
constexpr size_t WS_Q = 512u << 10;
constexpr size_t WS_Y = 1u << 20;

constexpr int L_SC = 0;
constexpr int L_QP = L_SC + 4 * 16 * 64;
constexpr int L_Q = L_QP + 4 * 4 * 32;
constexpr int L_OP = L_Q + 64;
constexpr int L_FP = L_OP + 4 * 128;
constexpr int L_MX = L_FP + 4 * 64;
constexpr int L_CW = L_MX + 4;
constexpr int L_CNT = L_CW + 4;
constexpr int L_END = L_CNT + 8;

__device__ __forceinline__ u32x4 ld_nt(const void* p) { return __builtin_nontemporal_load((const u32x4*)p); }
__device__ __forceinline__ float dot2(unsigned a, unsigned b, float acc) {
  return __builtin_amdgcn_fdot2_f32_bf16(__builtin_bit_cast(bf16x2, a), __builtin_bit_cast(bf16x2, b), acc, false);
}
__device__ __forceinline__ float bf_lo(unsigned v) { return __builtin_bit_cast(float, v << 16); }
__device__ __forceinline__ float bf_hi(unsigned v) { return __builtin_bit_cast(float, v & 0xffff0000u); }
__device__ __forceinline__ unsigned pack_bf16(float lo, float hi) {
  bf16x2 r = {(__bf16)lo, (__bf16)hi};
  return __builtin_bit_cast(unsigned, r);
}
__device__ __forceinline__ float rbf(float v) { return bf_lo(pack_bf16(v, 0.f)); }
template <int CTRL> __device__ __forceinline__ float dpp(float x) {
  return __builtin_bit_cast(float, __builtin_amdgcn_mov_dpp(__builtin_bit_cast(int, x), CTRL, 0xf, 0xf, true));
}
template <int CTRL> __device__ __forceinline__ unsigned dppu(unsigned x) {
  return (unsigned)__builtin_amdgcn_mov_dpp((int)x, CTRL, 0xf, 0xf, true);
}
constexpr int XOR1 = 0xB1, XOR2 = 0x4E, XOR3 = 0x1B, XOR7 = 0x141, ROR8 = 0x128, BCAST = 0x150;
__device__ __forceinline__ float rs32(float a, float b) {
  auto s = __builtin_amdgcn_permlane32_swap(__float_as_uint(a), __float_as_uint(b), false, false);
  return __uint_as_float(s[0]) + __uint_as_float(s[1]);
}
__device__ __forceinline__ float rs16(float a, float b) {
  auto s = __builtin_amdgcn_permlane16_swap(__float_as_uint(a), __float_as_uint(b), false, false);
  return __uint_as_float(s[0]) + __uint_as_float(s[1]);
}
__device__ __forceinline__ float wave_max(float x) {
  x = fmaxf(x, dpp<XOR1>(x)); x = fmaxf(x, dpp<XOR2>(x)); x = fmaxf(x, dpp<XOR7>(x)); x = fmaxf(x, dpp<ROR8>(x));
  auto s = __builtin_amdgcn_permlane16_swap(__float_as_uint(x), __float_as_uint(x), false, false);
  x = fmaxf(__uint_as_float(s[0]), __uint_as_float(s[1]));
  auto t = __builtin_amdgcn_permlane32_swap(__float_as_uint(x), __float_as_uint(x), false, false);
  return fmaxf(__uint_as_float(t[0]), __uint_as_float(t[1]));
}
__device__ __forceinline__ float wave_sum(float x) {
  x += dpp<XOR1>(x); x += dpp<XOR2>(x); x += dpp<XOR7>(x); x += dpp<ROR8>(x);
  x = rs16(x, x);
  return rs32(x, x);
}
__device__ __forceinline__ f32x2 row_reduce_scatter(const float (&acc)[8]) {
  const float u0 = rs32(acc[0], acc[4]), u1 = rs32(acc[1], acc[5]), u2 = rs32(acc[2], acc[6]), u3 = rs32(acc[3], acc[7]);
  return f32x2{rs16(u0, u2), rs16(u1, u3)};
}
__device__ __forceinline__ f32x2 row_reduce_scatter4(const float (&acc)[8]) {
  const float u0 = rs32(acc[0], acc[2]), u1 = rs32(acc[1], acc[3]), u4 = rs32(acc[4], acc[6]), u5 = rs32(acc[5], acc[7]);
  return f32x2{rs16(u0, u1), rs16(u4, u5)};
}
__device__ __forceinline__ void fma8(float (&acc)[8], float p, const u32x4& v) {
#pragma unroll
  for (int k = 0; k < 4; ++k) { acc[2 * k] = fmaf(p, bf_lo(v[k]), acc[2 * k]); acc[2 * k + 1] = fmaf(p, bf_hi(v[k]), acc[2 * k + 1]); }
}
#define LDS_FENCE() asm volatile("s_waitcnt lgkmcnt(0)" ::: "memory")
#define SB() __builtin_amdgcn_sched_barrier(0)
#define BARRIER() do { SB(); asm volatile("s_waitcnt lgkmcnt(0)" ::: "memory"); __builtin_amdgcn_s_barrier(); asm volatile("" ::: "memory"); SB(); } while (0)

template <int I> struct IC { static constexpr int v = I; };
template <int N, int I = 0, class F> __device__ __forceinline__ void static_for(F&& f) {
  if constexpr (I < N) { f(IC<I>{}); static_for<N, I + 1>(f); }
}

__device__ __forceinline__ void pace(unsigned t0, unsigned due) {
  asm volatile("1:\n\ts_memrealtime s[40:41]\n\ts_waitcnt lgkmcnt(0)\n\ts_sub_u32 s40, s40, %0\n\ts_cmp_lt_u32 s40, %1\n\ts_cbranch_scc1 1b"
               :: "s"(t0), "s"(due) : "s40", "s41", "scc");
}
struct Sched { unsigned t0, due, tau, xq, lo; };
template <int Q> __device__ __forceinline__ void issue_tick(u32x4 (&buf)[16], const char* group, Sched& sd) {
  SB(); pace(sd.t0, sd.due >> 8); SB();
  const char* pt = group + ((Q * 16384u) ^ sd.xq);
  static_for<4>([&](auto jj) {
    constexpr int j = decltype(jj)::v;
    buf[4 * Q + j] = ld_nt(pt + sd.lo + ((j & 1) * 512 + (j >> 1) * 4096 - 4096));
    SB();
  });
  sd.due += sd.tau;
}
template <int q, bool CUR, bool NXT>
__device__ __forceinline__ void refill(u32x4 (&buf)[16], const char* cur, const char* nxt, Sched& sd) {
  if constexpr (q + LAG < 4) { if constexpr (CUR) issue_tick<q + LAG>(buf, cur, sd); }
  else if constexpr (NXT) issue_tick<q + LAG - 4>(buf, nxt, sd);
}
struct Nothing { template <class T> __device__ __forceinline__ void operator()(T) const {} };
template <bool CUR, bool NXT, class F>
__device__ __forceinline__ float k_group(u32x4 (&buf)[16], const u32x4& q, const char* cur, const char* nxt, int c, Sched& sd, F&& after_tick) {
  float x[16];
  static_for<16>([&](auto tt) {
    constexpr int t = decltype(tt)::v;
    float d = dot2(buf[t][0], q[0], 0.f);
    d = dot2(buf[t][1], q[1], d); d = dot2(buf[t][2], q[2], d); d = dot2(buf[t][3], q[3], d);
    asm volatile("" : "+v"(d));
    x[t] = d;
    SB();
    if constexpr ((t & 3) == 3) { refill<(t >> 2), CUR, NXT>(buf, cur, nxt, sd); after_tick(IC<(t >> 2)>{}); }
  });
#define TR_STEP(HALF, CTRL, BIT) _Pragma("unroll") for (int j = 0; j < (HALF); ++j) { \
    const float keep = (c & (BIT)) ? x[j + (HALF)] : x[j], send = (c & (BIT)) ? x[j] : x[j + (HALF)]; x[j] = keep + dpp<CTRL>(send); }
  TR_STEP(8, ROR8, 8) TR_STEP(4, XOR7, 4) TR_STEP(2, XOR2, 2) TR_STEP(1, XOR1, 1)
#undef TR_STEP
  return rbf(rbf(x[0]) * 0.088388347648318440f);
}
template <bool NXT>
__device__ __forceinline__ void v_group(u32x4 (&buf)[16], float (&acc)[8], float p, const char* cur, const char* nxt, Sched& sd) {
  static_for<16>([&](auto tt) {
    constexpr int t = decltype(tt)::v;
    fma8(acc, dpp<BCAST + t>(p), buf[t]);
    asm volatile("" : "+v"(acc[0]), "+v"(acc[1]), "+v"(acc[2]), "+v"(acc[3]), "+v"(acc[4]), "+v"(acc[5]), "+v"(acc[6]), "+v"(acc[7]));
    SB();
    if constexpr ((t & 3) == 3) refill<(t >> 2), true, NXT>(buf, cur, nxt, sd);
  });
}
__device__ __forceinline__ u64 granule(unsigned tag, float v0, float v1) {
  const unsigned a = ((__float_as_uint(v0) + 0x80u) & 0xffffff00u) | (tag & 0xffu);
  const unsigned b = ((__float_as_uint(v1) + 0x80u) & 0xffffff00u) | ((tag >> 8) & 0xffu);
  return ((u64)b << 32) | a;
}
__device__ __forceinline__ void granule_take(unsigned tag, u64 g, f32x2& acc, unsigned& bad) {
  const unsigned a = (unsigned)g ^ (tag & 0xffu), b = (unsigned)(g >> 32) ^ ((tag >> 8) & 0xffu);
  bad |= a | b;
  acc += f32x2{__uint_as_float(a), __uint_as_float(b)};
}
__device__ __forceinline__ void lds_wait(unsigned* w, unsigned n, bool nap) {
#pragma unroll 1
  for (unsigned spins = 0; spins < (SPIN_CAP << 4); ++spins) {
    if (__hip_atomic_load(w, RLX_WG) >= n) break;
    if (nap) __builtin_amdgcn_s_sleep(8);
  }
  asm volatile("" ::: "memory");
}

__global__ void __launch_bounds__(NTHR, 2)
fused_decode_kernel(const unsigned short* __restrict__ x, const char* __restrict__ kc, const char* __restrict__ vc,
                    const char* __restrict__ wq, const char* __restrict__ wo, const char* __restrict__ w1,
                    const char* __restrict__ w3, const char* __restrict__ w2, const unsigned short* __restrict__ gw,
                    unsigned short* __restrict__ out, char* __restrict__ ws) {
  __shared__ __attribute__((aligned(16))) float lds[L_END];
  unsigned* ldsu = (unsigned*)lds;
  const int tid = threadIdx.x, lane = tid & 63, wave = __builtin_amdgcn_readfirstlane(tid >> 6);
  const int r = lane >> 4, c = lane & 15;
  const int bid = blockIdx.x, xg = bid & 7, idx = bid >> 3;
  const int h = xg * 4 + ((idx >> 1) & 3), b = idx >> 3, s = idx & 1;
  const int p = h * 2 + s;
  u64 code0;
  asm volatile("s_getpc_b64 %0" : "=s"(code0));

  const int hw = wave - 4, m = b * 2 + s, cq = m & 3, rp = m >> 2;
  u32x4 wv4[4]; u32x2 xp[4]; u64 ep = 0;
  const unsigned k0 = rp * 256 + (hw & 3) * 64 + 4 * (lane >> 2);
  unsigned wqo[4], xo = k0 * 2;
#pragma unroll
  for (int i = 0; i < 4; ++i) wqo[i] = ((k0 + i) * (NH * DH) + h * DH + 32 * cq + 8 * (lane & 3)) * 2;
  asm volatile("" : "+v"(wqo[0]), "+v"(wqo[1]), "+v"(wqo[2]), "+v"(wqo[3]), "+v"(xo));
  asm volatile("" :: "s"(x), "s"(kc), "s"(vc), "s"(wq), "s"(wo), "s"(w1), "s"(w3), "s"(w2), "s"(gw), "s"(out), "s"(ws));
  if (wave >= 4) {
    __builtin_amdgcn_s_setprio(3);
#pragma unroll
    for (int i = 0; i < 4; ++i) wv4[i] = ld_nt(wq + wqo[i]);
#pragma unroll
    for (int bb = 0; bb < 4; ++bb) xp[bb] = *(const u32x2*)((const char*)x + xo + bb * DM * 2);
    ep = __hip_atomic_load((GAS u64*)(ws + WS_EPOCH), RLX_AGENT);
    if (tid == 256) { ldsu[L_CNT + 0] = 0u; ldsu[L_CNT + 1] = 0u; ldsu[L_CNT + 2] = 0u; }
  }
  SB();
  u64 now64;
  asm volatile("s_memrealtime %0\n\ts_waitcnt lgkmcnt(0)" : "=s"(now64));
  const unsigned t0 = (unsigned)now64;
  if (idx == 0 && tid == 0) __hip_atomic_store((GAS unsigned*)(ws + WS_T0) + xg, t0, RLX_AGENT);
  BARRIER();

  if (__builtin_expect(wave < 4, 1)) {
    const unsigned chunk = (b * NH + h) * 2 + s;
    const char* kp = kc + (size_t)chunk * 1048576;
    const char* vp = vc + (size_t)chunk * 1048576;
    unsigned xm = 0;
#pragma unroll
    for (int i = 0; i < 8; ++i) xm ^= ((chunk >> i) & 1u) ? (unsigned)((XM >> (8 * i)) & 63u) : 0u;
    auto goff = [&](int g) { return ((g ^ (xm >> 2)) & 15) * 65536; };
    const unsigned phase = (xg & 1) ? PH : 0u;
    u32x4 pre[NPRE][16], buf[16];
    Sched sd{t0, (unsigned)((TDELTA) * 25600.0), TAU + (unsigned)((TEXTRA) * 25600.0 / (4 * NPRE + 4 + LAG)), (xm & 3u) << 14,
             (unsigned)(wave * 1024 + (r & 1) * 256 + (r >> 1) * 8192 + 4096 + c * 16)};
    if (wave == 3 && idx < CODE_KIB) {
      const u64 cp = (code0 & ~255ull) + idx * 1024;
      unsigned cw[16];
      asm volatile("s_load_dword %0, %16, 0x0\n\ts_load_dword %1, %16, 0x40\n\ts_load_dword %2, %16, 0x80\n\ts_load_dword %3, %16, 0xc0\n\t"
                   "s_load_dword %4, %16, 0x100\n\ts_load_dword %5, %16, 0x140\n\ts_load_dword %6, %16, 0x180\n\ts_load_dword %7, %16, 0x1c0\n\t"
                   "s_load_dword %8, %16, 0x200\n\ts_load_dword %9, %16, 0x240\n\ts_load_dword %10, %16, 0x280\n\ts_load_dword %11, %16, 0x2c0\n\t"
                   "s_load_dword %12, %16, 0x300\n\ts_load_dword %13, %16, 0x340\n\ts_load_dword %14, %16, 0x380\n\ts_load_dword %15, %16, 0x3c0\n\t"
                   "s_waitcnt lgkmcnt(0)"
                   : "=&s"(cw[0]), "=&s"(cw[1]), "=&s"(cw[2]), "=&s"(cw[3]), "=&s"(cw[4]), "=&s"(cw[5]), "=&s"(cw[6]), "=&s"(cw[7]),
                     "=&s"(cw[8]), "=&s"(cw[9]), "=&s"(cw[10]), "=&s"(cw[11]), "=&s"(cw[12]), "=&s"(cw[13]), "=&s"(cw[14]), "=&s"(cw[15])
                   : "s"(cp));
    }
    unsigned pf0 = 0, pf1 = 0, pf2 = 0, pf3 = 0;
    {
      const char* pa = wave < 2 ? wo + (size_t)xg * 524288 + idx * 16384 + wave * 8192 + lane * 128
                                : (wave == 2 ? w1 : w3) + (size_t)(16 * idx + (lane >> 2)) * (DF * 2) + xg * 512 + (lane & 3) * 128;
      pace(t0, wave < 2 ? steps(PF_A) : steps(PF_B));
      if (wave < 2 || xg < 7) {
        pf0 = __builtin_nontemporal_load((const unsigned*)pa); SB();
        pf1 = __hip_atomic_load((GAS unsigned*)pa, RLX_AGENT);
      }
      SB();
      if (wave == 1 && lane < 2) pf2 = __builtin_nontemporal_load((const unsigned*)(lane ? vp : kp));
      if (wave == 0 && xg < 7) {
        const char* pb = w2 + (size_t)xg * 262144 + idx * 8192 + lane * 128;
        pace(t0, steps(PF_C)); SB();
        pf2 = __builtin_nontemporal_load((const unsigned*)pb); SB();
        pf3 = __hip_atomic_load((GAS unsigned*)pb, RLX_AGENT);
      }
      SB();
    }
    {
      asm volatile("" :: "s"(xm), "s"(sd.xq), "v"(sd.lo), "s"(kp + goff(0)));
      pace(t0, steps(TREAD));
      u32x8 bc;
      asm volatile("s_load_dwordx8 %0, %1, 0x0\n\ts_waitcnt lgkmcnt(0)" : "=&s"(bc) : "s"(ws + WS_T0) : "memory");
      unsigned emax = 0u;
#pragma unroll
      for (int i = 0; i < 8; ++i) {
        const unsigned e = bc[i] - (t0 - 2000u);
        emax = max(emax, e < 2000u + steps(TREAD) ? e : 0u);
        asm volatile("" : "+s"(emax));
      }
      sd.t0 = t0 + (emax ? emax - 2000u : 0u) + phase;
      if (tid == 0) ldsu[L_CNT + 3] = sd.t0;
    }
    static_for<NPRE>([&](auto gg) {
      constexpr int g = decltype(gg)::v;
      issue_tick<0>(pre[g], kp + goff(g), sd); issue_tick<1>(pre[g], kp + goff(g), sd);
      issue_tick<2>(pre[g], kp + goff(g), sd); issue_tick<3>(pre[g], kp + goff(g), sd);
    });
    issue_tick<0>(buf, kp + goff(NPRE), sd); issue_tick<1>(buf, kp + goff(NPRE), sd);
    issue_tick<2>(buf, kp + goff(NPRE), sd); issue_tick<3>(buf, kp + goff(NPRE), sd);
    BARRIER();
    asm volatile("" :: "v"(pf0), "v"(pf1), "v"(pf2), "v"(pf3));

    const u32x4 q = *(const u32x4*)(lds + L_Q + 4 * c);
    float* sc = lds + L_SC + wave * 1024 + lane;
    float mx = -INFINITY;
    {
      const float sv = k_group<false, true>(buf, q, kp, kp + goff(NPRE + 1), c, sd, [&](auto qq) {
        constexpr int g = decltype(qq)::v - (3 - NPRE);
        if constexpr (g >= 0 && g < NPRE) {
          const float pv = k_group<false, false>(pre[g], q, kp, kp, c, sd, Nothing{});
          sc[g * 64] = pv;
          mx = fmaxf(mx, pv);
          SB();
        }
      });
      sc[NPRE * 64] = sv;
      mx = fmaxf(mx, sv);
    }
    sd.tau = TAU;
#pragma unroll 1
    for (int g = NPRE + 1; g < 16; ++g) {
      const char* cur = kp + goff(g);
      const char* nxt = ((g < 15) ? kp : vp) + goff(g + 1);
      const float sv = k_group<true, true>(buf, q, cur, nxt, c, sd, Nothing{});
      sc[g * 64] = sv;
      mx = fmaxf(mx, sv);
    }
    float sn = sc[0];
    mx = wave_max(mx);
    if (lane == 0) {
      lds[L_MX + wave] = mx;
      LDS_FENCE();
      __hip_atomic_fetch_add(ldsu + L_CNT + 0, 1u, RLX_WG);
    }
    float acc[8];
#pragma unroll
    for (int k = 0; k < 8; ++k) acc[k] = 0.f;
#pragma unroll 1
    for (int g = 0; g < 15; ++g) {
      const float pc = __builtin_amdgcn_exp2f((sn - mx) * LOG2E);
      sn = sc[(g + 1) * 64];
      v_group<true>(buf, acc, pc, vp + goff(g), vp + goff(g + 1), sd);
    }
    v_group<false>(buf, acc, __builtin_amdgcn_exp2f((sn - mx) * LOG2E), vp + goff(15), vp, sd);
    *(f32x2*)(lds + L_OP + wave * 128 + 2 * lane) = row_reduce_scatter4(acc);
    BARRIER();
    if (wave == 0 && s == 0) {
      const unsigned tbar = (unsigned)__builtin_amdgcn_s_memrealtime() - sd.t0 + steps(YWAIT);
      const unsigned tlast = ((sd.due - sd.tau) >> 8) - phase + steps(YSCHED);
      const unsigned t_early = max(tlast + YEARLY, phase ? 0u : tbar), t_late = max(tlast + PH, tbar);
      ep = ((u64)ldsu[L_CNT + 5] << 32) | ldsu[L_CNT + 4];
      const unsigned tag = __builtin_amdgcn_readfirstlane((unsigned)ep);
      GAS u64* yin = (GAS u64*)(ws + WS_Y) + ((size_t)(b * 32 + h) * 64) * 8 + lane;
      GAS unsigned short* op = (GAS unsigned short*)out + b * DM + 128 * (h >> 3) + 8 * (8 * (h & 1) + (lane & 7)) + 2 * ((h >> 1) & 3) + (lane >> 5);
      asm volatile("" : "+v"(yin), "+v"(op));
      u64 gv[8];
      f32x2 sm; unsigned bad;
      auto take = [&](int odd) {
#pragma unroll
        for (int i = 0; i < 4; ++i) { granule_take(tag, gv[2 * i + odd], sm, bad); asm volatile("" : "+v"(sm), "+v"(bad)); }
      };
      SB(); pace(sd.t0, t_early); SB();
#pragma unroll
      for (int i = 0; i < 8; i += 2) gv[i] = __hip_atomic_load(yin + i * 64, RLX_AGENT);
      SB(); pace(sd.t0, t_late); SB();
#pragma unroll
      for (int i = 1; i < 8; i += 2) gv[i] = __hip_atomic_load(yin + i * 64, RLX_AGENT);
      SB();
      sm = f32x2{0.f, 0.f}; bad = 0u;
      take(0);
      SB();
      take(1);
      bool ok = __all((bad & 0xffu) == 0u);
      if (__builtin_expect(!ok, 0)) {
#pragma unroll 1
        for (unsigned spins = 0; spins < SPIN_CAP && !ok; ++spins) {
#pragma unroll
          for (int i = 0; i < 8; ++i) gv[i] = __hip_atomic_load(yin + i * 64, RLX_AGENT);
          sm = f32x2{0.f, 0.f}; bad = 0u;
          take(0); take(1);
          ok = __all((bad & 0xffu) == 0u);
        }
        if (!ok) sm[0] = sm[1] = __uint_as_float(0x7fc00000u);
      }
      float sum = rs32(sm[0], sm[1]);
      sum = rs16(sum, sum);
      sum += dpp<ROR8>(sum);
      if ((lane & 31) < 8) __hip_atomic_store(op, (unsigned short)pack_bf16(sum, 0.f), RLX_AGENT);
      if (bid == 0 && lane == 0) __hip_atomic_store((GAS u64*)(ws + WS_EPOCH), ep, RLX_AGENT);
    }
  } else {
    const unsigned short* xb = x + b * DM;
    {
      float a[32];
#pragma unroll
      for (int k = 0; k < 4; ++k) {
        const unsigned l0 = __builtin_amdgcn_perm(wv4[1][k], wv4[0][k], 0x05040100u), h0 = __builtin_amdgcn_perm(wv4[1][k], wv4[0][k], 0x07060302u);
        const unsigned l1 = __builtin_amdgcn_perm(wv4[3][k], wv4[2][k], 0x05040100u), h1 = __builtin_amdgcn_perm(wv4[3][k], wv4[2][k], 0x07060302u);
#pragma unroll
        for (int bb = 0; bb < 4; ++bb) {
          a[bb * 8 + 2 * k] = dot2(l1, xp[bb][1], dot2(l0, xp[bb][0], 0.f));
          a[bb * 8 + 2 * k + 1] = dot2(h1, xp[bb][1], dot2(h0, xp[bb][0], 0.f));
        }
      }
#pragma unroll
      for (int j = 0; j < 16; ++j) a[j] = rs32(a[j], a[j + 16]);
#pragma unroll
      for (int j = 0; j < 8; ++j) a[j] = rs16(a[j], a[j + 8]);
#pragma unroll
      for (int j = 0; j < 4; ++j) { const float keep = (lane & 8) ? a[j + 4] : a[j], send = (lane & 8) ? a[j] : a[j + 4]; a[j] = keep + dpp<ROR8>(send); }
#pragma unroll
      for (int j = 0; j < 2; ++j) { const float keep = (lane & 4) ? a[j + 2] : a[j], send = (lane & 4) ? a[j] : a[j + 2]; a[j] = keep + dpp<XOR3>(dpp<XOR7>(send)); }
      *(f32x2*)(lds + L_QP + hw * 128 + r * 32 + 8 * (lane & 3) + (c >> 2) * 2) = f32x2{a[0], a[1]};
      asm volatile("s_waitcnt vmcnt(0) lgkmcnt(0)" : "+v"(ep) :: "memory");
      ep += 1;
      if (lane == 0) __hip_atomic_fetch_add(ldsu + L_CNT + 2, 1u, RLX_WG);
      if (hw == 0) {
        lds_wait(ldsu + L_CNT + 2, 4u, false);
        const float* qp = lds + L_QP + 2 * lane;
        const f32x2 qv = (*(const f32x2*)(qp) + *(const f32x2*)(qp + 128)) + (*(const f32x2*)(qp + 256) + *(const f32x2*)(qp + 384));
        GAS u64* qx = (GAS u64*)(ws + WS_Q);
        __hip_atomic_store(qx + ((h * 4 + r) * 2 + rp) * 64 + 16 * cq + c, granule((unsigned)ep, qv[0], qv[1]), RLX_AGENT);
        pace(t0, steps(QWAIT));
        f32x2 qs = {0.f, 0.f}; bool ok = false;
#pragma unroll 1
        for (unsigned spins = 0; spins < SPIN_CAP; ++spins) {
          const u64 g0 = __hip_atomic_load(qx + (h * 4 + b) * 128 + lane, RLX_AGENT), g1 = __hip_atomic_load(qx + (h * 4 + b) * 128 + 64 + lane, RLX_AGENT);
          unsigned bad = 0u;
          qs = f32x2{0.f, 0.f};
          granule_take((unsigned)ep, g0, qs, bad); granule_take((unsigned)ep, g1, qs, bad);
          ok = __all((bad & 0xffu) == 0u);
          if (ok) break;
        }
        if (!ok) qs[0] = __uint_as_float(0x7fc00000u);
        ldsu[L_Q + lane] = pack_bf16(qs[0], qs[1]);
        if (lane == 0) { ldsu[L_CNT + 4] = (unsigned)ep; ldsu[L_CNT + 5] = (unsigned)(ep >> 32); }
      }
    }
    __builtin_amdgcn_s_setprio(0);
    BARRIER();
    float g, og;
    {
      const u32x4 xv = *(const u32x4*)(xb + 8 * lane), gv = *(const u32x4*)(gw + 8 * lane);
      float z = 0.f;
#pragma unroll
      for (int k = 0; k < 4; ++k) z += rbf(bf_lo(xv[k]) * bf_lo(gv[k])) + rbf(bf_hi(xv[k]) * bf_hi(gv[k]));
      z = rbf(wave_sum(z));
      g = rbf(1.f / (1.f + __expf(-z)));
      og = rbf(1.f - g);
    }
    const unsigned tb = __builtin_amdgcn_readfirstlane(ldsu[L_CNT + 3]) + hw * (unsigned)(HSTAG * TAU / 256.0);
    constexpr double H0 = 4 * NPRE + 6.5;
    auto hdue = [](double ticks) constexpr { return (unsigned)(ticks * TAU / 256.0 + ((TDELTA) + (TEXTRA)) * 100.0); };
    float yacc[8];
#pragma unroll
    for (int k = 0; k < 8; ++k) yacc[k] = 0.f;
    if (p < DF / 32) {
      const int r16 = lane >> 2, c4 = lane & 3;
      const size_t fo = ((size_t)(32 * r16 + hw) * DF + 32 * p + 8 * c4) * 2;
      u32x4 a[8], u[8], dv[8]; float xk[8];
#pragma unroll
      for (int i = 0; i < 8; ++i) xk[i] = bf_lo(xb[32 * r16 + 4 * i + hw]);
      static_for<8>([&](auto ii) {
        constexpr int i = decltype(ii)::v;
        SB(); pace(tb, hdue(H0 + (3 * i + 0) * HSP_F)); SB(); a[i] = ld_nt(w1 + fo + (size_t)i * 4 * DF * 2);
        SB(); pace(tb, hdue(H0 + (3 * i + 1) * HSP_F)); SB(); u[i] = ld_nt(w3 + fo + (size_t)i * 4 * DF * 2);
        SB(); pace(tb, hdue(H0 + (3 * i + 2) * HSP_F)); SB(); dv[i] = ld_nt(w2 + ((size_t)(32 * p + 4 * (i ^ (p & 7)) + r) * DM + 128 * hw + 8 * c) * 2);
        SB();
      });
      float ga[8], ua[8];
#pragma unroll
      for (int k = 0; k < 8; ++k) { ga[k] = 0.f; ua[k] = 0.f; }
#pragma unroll
      for (int i = 0; i < 8; ++i) { fma8(ga, xk[i], a[i]); fma8(ua, xk[i], u[i]); }
#pragma unroll
      for (int k = 0; k < 8; ++k) {
#pragma unroll
        for (int w = 4; w < 64; w <<= 1) { ga[k] += __shfl_xor(ga[k], w); ua[k] += __shfl_xor(ua[k], w); }
      }
      if (lane < 4) {
#pragma unroll
        for (int k = 0; k < 8; ++k) { lds[L_FP + hw * 64 + 8 * lane + k] = ga[k]; lds[L_FP + hw * 64 + 32 + 8 * lane + k] = ua[k]; }
      }
      LDS_FENCE();
      if (lane == 0) __hip_atomic_fetch_add(ldsu + L_CNT + 1, 1u, RLX_WG);
      lds_wait(ldsu + L_CNT + 1, 4u, true);
      const int f = 4 * ((c ^ p) & 7) + r;
      const float gt = rbf((lds[L_FP + f] + lds[L_FP + 64 + f]) + (lds[L_FP + 128 + f] + lds[L_FP + 192 + f]));
      const float up = rbf((lds[L_FP + 32 + f] + lds[L_FP + 96 + f]) + (lds[L_FP + 160 + f] + lds[L_FP + 224 + f]));
      const float hv = og * rbf(rbf(gt / (1.f + __expf(-gt))) * up);
      static_for<8>([&](auto ii) { constexpr int i = decltype(ii)::v; fma8(yacc, dpp<BCAST + i>(hv), dv[i]); });
    }
    GAS u64* ml = (GAS u64*)(ws + WS_ML);
    const unsigned tag = (unsigned)ep;
    float mw = 0.f, lw = 0.f;
    if (hw == 0) {
      lds_wait(ldsu + L_CNT + 0, 4u, true);
      const f32x4 m4 = *(const f32x4*)(lds + L_MX);
      mw = fmaxf(fmaxf(m4[0], m4[1]), fmaxf(m4[2], m4[3]));
#pragma unroll 4
      for (int i = 0; i < 16; ++i) {
        const f32x4 sv = *(const f32x4*)(lds + L_SC + i * 256 + 4 * lane);
#pragma unroll
        for (int k = 0; k < 4; ++k) lw += __builtin_amdgcn_exp2f((sv[k] - mw) * LOG2E);
      }
      lw = wave_sum(lw);
      if (lane < 2) __hip_atomic_store(ml + (b * 64 + p) * 16 + lane, ((u64)tag << 32) | __float_as_uint(lane ? lw : mw), RLX_AGENT);
    }
    u32x4 bf[8][4];
    {
      u32x4 wv[32];
      const char* wp = wo + ((size_t)(h * DH + r) * DM + 128 * hw + 8 * c) * 2;
      static_for<32>([&](auto ii) {
        constexpr int i = decltype(ii)::v;
        SB(); pace(tb, hdue(H0 + 24 * HSP_F + i * HSP_O)); SB(); wv[i] = ld_nt(wp + (size_t)(i ^ (h & 7)) * 4 * DM * 2); SB();
      });
#pragma unroll
      for (int ks = 0; ks < 4; ++ks) {
#pragma unroll
        for (int k = 0; k < 4; ++k) {
#pragma unroll
          for (int qd = 0; qd < 4; ++qd) {
            const unsigned e0 = wv[8 * ks + 2 * qd][k], e1 = wv[8 * ks + 2 * qd + 1][k];
            bf[2 * k][ks][qd] = __builtin_amdgcn_perm(e1, e0, 0x05040100u);
            bf[2 * k + 1][ks][qd] = __builtin_amdgcn_perm(e1, e0, 0x07060302u);
          }
        }
#pragma unroll
        for (int t = 0; t < 8; ++t) asm volatile("" : "+v"(bf[t][ks]));
      }
    }
    f32x2 yf = row_reduce_scatter(yacc);
    asm volatile("" : "+v"(yf));
    if (hw == 0) {
      float mo = __uint_as_float(0x7fc00000u), lo = mo;
#pragma unroll 1
      for (unsigned spins = 0; spins < SPIN_CAP; ++spins) {
        const u64 o0 = __hip_atomic_load(ml + (b * 64 + (p ^ 1)) * 16, RLX_AGENT), o1 = __hip_atomic_load(ml + (b * 64 + (p ^ 1)) * 16 + 1, RLX_AGENT);
        if ((unsigned)(o0 >> 32) == tag && (unsigned)(o1 >> 32) == tag) { mo = __uint_as_float((unsigned)o0); lo = __uint_as_float((unsigned)o1); break; }
        __builtin_amdgcn_s_sleep(8);
      }
      const float mt = fmaxf(mw, mo);
      const float lt = lw * __builtin_amdgcn_exp2f((mw - mt) * LOG2E) + lo * __builtin_amdgcn_exp2f((mo - mt) * LOG2E);
      if (lane < 4) lds[L_CW + lane] = g * __builtin_amdgcn_exp2f((lds[L_MX + lane] - mt) * LOG2E) / lt;
    }
    GAS u64* yo = (GAS u64*)(ws + WS_Y) + ((size_t)(b * 32 + (hw * 4 + r) * 2 + (c >> 3)) * 64 + p) * 8 + (c & 7);
    asm volatile("" : "+v"(yo));
    const bool rodd = (lane & 16) != 0, rhi = (lane & 32) != 0;
    const float* op_src = lds + L_OP + 2 * (lane ^ ((h & 7) >> 1));
    __builtin_amdgcn_s_setprio(3);
    BARRIER();
    {
      const f32x4 cw = *(const f32x4*)(lds + L_CW);
      f32x2 o = cw[0] * *(const f32x2*)(op_src);
#pragma unroll
      for (int w = 1; w < 4; ++w) o += cw[w] * *(const f32x2*)(op_src + w * 128);
      const unsigned opk = pack_bf16(o[0], o[1]);
      const unsigned op = __builtin_amdgcn_alignbit(opk, opk, (h & 1) * 16);
      f32x4 d[8];
#pragma unroll
      for (int t = 0; t < 8; ++t) d[t] = f32x4{0.f, 0.f, 0.f, 0.f};
      u32x4 af[4];
      af[0] = u32x4{dppu<BCAST + 0>(op), dppu<BCAST + 1>(op), dppu<BCAST + 2>(op), dppu<BCAST + 3>(op)};
      SB();
      static_for<4>([&](auto kk) {
        constexpr int ks = decltype(kk)::v;
        static_for<8>([&](auto tt) {
          constexpr int t = decltype(tt)::v;
          d[t] = __builtin_amdgcn_mfma_f32_16x16x32_bf16(__builtin_bit_cast(bf16x8, af[ks]), __builtin_bit_cast(bf16x8, bf[t][ks]), d[t], 0, 0, 0);
          if constexpr (ks < 3 && t < 4) af[ks + 1][t] = dppu<BCAST + 4 * ks + 4 + t>(op);
          SB();
        });
      });
      const float y0 = yf[0] + (rodd ? (rhi ? d[6][0] : d[2][0]) : (rhi ? d[4][0] : d[0][0]));
      const float y1 = yf[1] + (rodd ? (rhi ? d[7][0] : d[3][0]) : (rhi ? d[5][0] : d[1][0]));
      __hip_atomic_store(yo, granule(tag, y0, y1), RLX_AGENT);
    }
  }
}

extern "C" void kernel_launch(void* const* d_in, const int* in_sizes, int n_in, void* d_out, int out_size, void* d_ws,
                              size_t ws_size, hipStream_t stream) {
  (void)in_sizes; (void)n_in; (void)out_size; (void)ws_size;
  fused_decode_kernel<<<NWG, NTHR, 0, stream>>>((const unsigned short*)d_in[0], (const char*)d_in[1], (const char*)d_in[2],
                                                (const char*)d_in[3], (const char*)d_in[4], (const char*)d_in[6],
                                                (const char*)d_in[7], (const char*)d_in[8], (const unsigned short*)d_in[9],
                                                (unsigned short*)d_out, (char*)d_ws);
}
```

```cpp
#include <hip/hip_runtime.h>
#include <hip/hip_bf16.h>
#include <stdint.h>

typedef unsigned u32x2 __attribute__((ext_vector_type(2)));
typedef unsigned u32x4 __attribute__((ext_vector_type(4)));
typedef unsigned u32x8 __attribute__((ext_vector_type(8)));
typedef float f32x2 __attribute__((ext_vector_type(2)));
typedef float f32x4 __attribute__((ext_vector_type(4)));
typedef __bf16 bf16x2 __attribute__((ext_vector_type(2)));
typedef __bf16 bf16x8 __attribute__((ext_vector_type(8)));
typedef unsigned long long u64;
#define GAS __attribute__((address_space(1)))
#define RLX_AGENT __ATOMIC_RELAXED, __HIP_MEMORY_SCOPE_AGENT
#define RLX_WG __ATOMIC_RELAXED, __HIP_MEMORY_SCOPE_WORKGROUP

constexpr int NH = 32, DH = 128, DM = 512, DF = 1792;
constexpr int NWG = 256, NTHR = 512;
constexpr unsigned SPIN_CAP = 1u << 18;
constexpr float LOG2E = 1.4426950408889634f;

constexpr double TSTREAM = 68.15;
constexpr double TDELTA = 1.6;
constexpr double TEXTRA = 0.55;
constexpr int NEXTRA = 10;
constexpr double YSCHED = 1.54;
constexpr double YWAIT = 0.55;
constexpr double QWAIT = 6.0;
constexpr double TREAD = 1.15;
constexpr double PF_A = 0.1, PF_B = 0.6, PF_C = 0.8;
constexpr double HSP_F = 2.4, HSP_O = 1.2, HSTAG = 0.33;
constexpr int LAG = 3;
constexpr int NPRE = 2;
constexpr unsigned PH_A = 13, PH_B = 27;
constexpr unsigned YEARLY = 30;
constexpr u64 XM = 0x0a111c100a030207ull;
constexpr unsigned TAU = (unsigned)(TSTREAM * 200.0);
constexpr unsigned steps(double us) { return (unsigned)(us * 100.0); }

constexpr size_t WS_EPOCH = 0;
constexpr size_t WS_T0 = 1024;
constexpr size_t WS_ML = 4096;
constexpr size_t WS_Q = 512u << 10;
constexpr size_t WS_Y = 1u << 20;

constexpr int L_SC = 0;
constexpr int L_QP = L_SC + 4 * 16 * 64;
constexpr int L_Q = L_QP + 4 * 4 * 32;
constexpr int L_OP = L_Q + 64;
constexpr int L_FP = L_OP + 4 * 128;
constexpr int L_MX = L_FP + 4 * 64;
constexpr int L_CW = L_MX + 4;
constexpr int L_CNT = L_CW + 4;
constexpr int L_END = L_CNT + 8;

__device__ __forceinline__ u32x4 ld_nt(const void* p) { return __builtin_nontemporal_load((const u32x4*)p); }
__device__ __forceinline__ float dot2(unsigned a, unsigned b, float acc) {
  return __builtin_amdgcn_fdot2_f32_bf16(__builtin_bit_cast(bf16x2, a), __builtin_bit_cast(bf16x2, b), acc, false);
}
__device__ __forceinline__ float bf_lo(unsigned v) { return __builtin_bit_cast(float, v << 16); }
__device__ __forceinline__ float bf_hi(unsigned v) { return __builtin_bit_cast(float, v & 0xffff0000u); }
__device__ __forceinline__ unsigned pack_bf16(float lo, float hi) {
  bf16x2 r = {(__bf16)lo, (__bf16)hi};
  return __builtin_bit_cast(unsigned, r);
}
__device__ __forceinline__ float rbf(float v) { return bf_lo(pack_bf16(v, 0.f)); }
template <int CTRL> __device__ __forceinline__ float dpp(float x) {
  return __builtin_bit_cast(float, __builtin_amdgcn_mov_dpp(__builtin_bit_cast(int, x), CTRL, 0xf, 0xf, true));
}
template <int CTRL> __device__ __forceinline__ unsigned dppu(unsigned x) {
  return (unsigned)__builtin_amdgcn_mov_dpp((int)x, CTRL, 0xf, 0xf, true);
}
constexpr int XOR1 = 0xB1, XOR2 = 0x4E, XOR3 = 0x1B, XOR7 = 0x141, ROR8 = 0x128, BCAST = 0x150;
__device__ __forceinline__ float rs32(float a, float b) {
  auto s = __builtin_amdgcn_permlane32_swap(__float_as_uint(a), __float_as_uint(b), false, false);
  return __uint_as_float(s[0]) + __uint_as_float(s[1]);
}
__device__ __forceinline__ float rs16(float a, float b) {
  auto s = __builtin_amdgcn_permlane16_swap(__float_as_uint(a), __float_as_uint(b), false, false);
  return __uint_as_float(s[0]) + __uint_as_float(s[1]);
}
__device__ __forceinline__ float wave_max(float x) {
  x = fmaxf(x, dpp<XOR1>(x)); x = fmaxf(x, dpp<XOR2>(x)); x = fmaxf(x, dpp<XOR7>(x)); x = fmaxf(x, dpp<ROR8>(x));
  auto s = __builtin_amdgcn_permlane16_swap(__float_as_uint(x), __float_as_uint(x), false, false);
  x = fmaxf(__uint_as_float(s[0]), __uint_as_float(s[1]));
  auto t = __builtin_amdgcn_permlane32_swap(__float_as_uint(x), __float_as_uint(x), false, false);
  return fmaxf(__uint_as_float(t[0]), __uint_as_float(t[1]));
}
__device__ __forceinline__ float wave_sum(float x) {
  x += dpp<XOR1>(x); x += dpp<XOR2>(x); x += dpp<XOR7>(x); x += dpp<ROR8>(x);
  x = rs16(x, x);
  return rs32(x, x);
}
__device__ __forceinline__ f32x2 row_reduce_scatter(const float (&acc)[8]) {
  const float u0 = rs32(acc[0], acc[4]), u1 = rs32(acc[1], acc[5]), u2 = rs32(acc[2], acc[6]), u3 = rs32(acc[3], acc[7]);
  return f32x2{rs16(u0, u2), rs16(u1, u3)};
}
__device__ __forceinline__ f32x2 row_reduce_scatter4(const f32x2 (&acc)[4]) {
  const float u0 = rs32(acc[0][0], acc[1][0]), u1 = rs32(acc[0][1], acc[1][1]), u4 = rs32(acc[2][0], acc[3][0]), u5 = rs32(acc[2][1], acc[3][1]);
  return f32x2{rs16(u0, u1), rs16(u4, u5)};
}
__device__ __forceinline__ void fma8(float (&acc)[8], float p, const u32x4& v) {
#pragma unroll
  for (int k = 0; k < 4; ++k) { acc[2 * k] = fmaf(p, bf_lo(v[k]), acc[2 * k]); acc[2 * k + 1] = fmaf(p, bf_hi(v[k]), acc[2 * k + 1]); }
}
#define LDS_FENCE() asm volatile("s_waitcnt lgkmcnt(0)" ::: "memory")
#define SB() __builtin_amdgcn_sched_barrier(0)
#define BARRIER() do { SB(); asm volatile("s_waitcnt lgkmcnt(0)" ::: "memory"); __builtin_amdgcn_s_barrier(); asm volatile("" ::: "memory"); SB(); } while (0)

template <int I> struct IC { static constexpr int v = I; };
template <int N, int I = 0, class F> __device__ __forceinline__ void static_for(F&& f) {
  if constexpr (I < N) { f(IC<I>{}); static_for<N, I + 1>(f); }
}

__device__ __forceinline__ void pace(unsigned t0, unsigned due) {
  asm volatile("1:\n\ts_memrealtime s[40:41]\n\ts_waitcnt lgkmcnt(0)\n\ts_sub_u32 s40, s40, %0\n\ts_cmp_lt_u32 s40, %1\n\ts_cbranch_scc1 1b"
               :: "s"(t0), "s"(due) : "s40", "s41", "scc");
}
__device__ __forceinline__ void pace_lazy(unsigned t0, unsigned due) {
  asm volatile("s_branch 2f\n1:\n\ts_sleep 4\n2:\n\ts_memrealtime s[40:41]\n\ts_waitcnt lgkmcnt(0)\n\ts_sub_u32 s40, s40, %0\n\t"
               "s_cmp_lt_u32 s40, %1\n\ts_cbranch_scc1 1b" :: "s"(t0), "s"(due) : "s40", "s41", "scc");
}
struct Sched { unsigned t0, due, tau, xq, lo, jump; };
constexpr unsigned NEVER = 0x40000000u;
template <int Q, bool STEADY> __device__ __forceinline__ void issue_tick(u32x4 (&buf)[16], const char* group, Sched& sd) {
  const char* pt = group + ((Q * 16384u) ^ sd.xq);
  SB();
  if constexpr (STEADY) {
    unsigned d;
    asm volatile("s_memrealtime s[40:41]\n\ts_lshr_b32 %2, %0, 8\n\ts_waitcnt lgkmcnt(0)\n\ts_sub_u32 s40, s40, %3\n\ts_sub_u32 s41, s40, %2\n\t"
                 "s_cmp_gt_i32 s41, %1\n\ts_cselect_b32 %2, %4, 0\n\ts_add_u32 %0, %0, %2\n\ts_cmp_lt_i32 s41, 20\n\ts_cselect_b32 %1, %5, %6\n\t"
                 "s_lshr_b32 %2, %0, 8\n\ts_cmp_ge_u32 s40, %2\n\ts_cbranch_scc1 2f\n"
                 "1:\n\ts_memrealtime s[40:41]\n\ts_waitcnt lgkmcnt(0)\n\ts_sub_u32 s40, s40, %3\n\ts_cmp_lt_u32 s40, %2\n\ts_cbranch_scc1 1b\n2:"
                 : "+s"(sd.due), "+s"(sd.jump), "=&s"(d) : "s"(sd.t0), "s"(5u * TAU), "s"(110u), "i"(NEVER) : "s40", "s41", "scc");
  } else {
    pace(sd.t0, sd.due >> 8);
  }
  SB();
  asm volatile("" : "+v"(sd.lo));
  static_for<4>([&](auto jj) {
    constexpr int j = decltype(jj)::v;
    buf[4 * Q + j] = ld_nt(pt + sd.lo + j * 1024);
    SB();
    if constexpr (j < 3) { asm volatile("s_nop 5"); SB(); }
  });
  sd.due += sd.tau;
}
template <int q, bool CUR, bool NXT>
__device__ __forceinline__ void refill(u32x4 (&buf)[16], const char* cur, const char* nxt, Sched& sd) {
  if constexpr (q + LAG < 4) { if constexpr (CUR) issue_tick<q + LAG, CUR>(buf, cur, sd); }
  else if constexpr (NXT) issue_tick<q + LAG - 4, CUR>(buf, nxt, sd);
}
struct Nothing { template <class T> __device__ __forceinline__ void operator()(T) const {} };
template <bool CUR, bool NXT, class F>
__device__ __forceinline__ float k_group(u32x4 (&buf)[16], const u32x4& q, const char* cur, const char* nxt, int c, Sched& sd, F&& after_tick) {
  auto mix = [&](float lo, float hi, int bit, auto ex) { const float keep = (c & bit) ? hi : lo, send = (c & bit) ? lo : hi; return keep + ex(send); };
  float y[4];
  static_for<4>([&](auto qq) {
    constexpr int tk = decltype(qq)::v;
    float d[4];
#pragma unroll
    for (int j = 0; j < 4; ++j) d[j] = dot2(buf[4 * tk + j][0], q[0], 0.f);
#pragma unroll
    for (int k = 1; k < 4; ++k)
#pragma unroll
      for (int j = 0; j < 4; ++j) d[j] = dot2(buf[4 * tk + j][k], q[k], d[j]);
    const float e0 = mix(d[0], d[1], 1, [](float v) { return dpp<XOR1>(v); }), e1 = mix(d[2], d[3], 1, [](float v) { return dpp<XOR1>(v); });
    y[tk] = mix(e0, e1, 2, [](float v) { return dpp<XOR2>(v); });
    if constexpr (tk >= 2) y[tk - 2] = mix(y[tk - 2], y[tk], 8, [](float v) { return dpp<ROR8>(v); });
    asm volatile("" : "+v"(y[tk & 1]));
    SB();
    refill<tk, CUR, NXT>(buf, cur, nxt, sd);
    after_tick(IC<tk>{});
  });
  return rbf(rbf(mix(y[0], y[1], 4, [](float v) { return dpp<XOR3>(dpp<XOR7>(v)); })) * 0.088388347648318440f);
}
template <bool NXT>
__device__ __forceinline__ void v_group(u32x4 (&buf)[16], f32x2 (&acc)[4], float p, const char* cur, const char* nxt, Sched& sd) {
  static_for<16>([&](auto tt) {
    constexpr int t = decltype(tt)::v;
    const float pb = dpp<BCAST + t>(p);
#pragma unroll
    for (int k = 0; k < 4; ++k) acc[k] = __builtin_elementwise_fma(f32x2{pb, pb}, f32x2{bf_lo(buf[t][k]), bf_hi(buf[t][k])}, acc[k]);
    asm volatile("" : "+v"(acc[0]), "+v"(acc[1]), "+v"(acc[2]), "+v"(acc[3]));
    SB();
    if constexpr ((t & 3) == 3) refill<(t >> 2), true, NXT>(buf, cur, nxt, sd);
  });
}
template <int TB> __device__ __forceinline__ u32x2 tag_of(u64 ep, int place) {
  constexpr unsigned M = (1u << TB) - 1u;
  return u32x2{(unsigned)ep & M, (unsigned)(ep >> (TB * min(place + 1, 64 / TB - 1))) & M};
}
template <int TB> __device__ __forceinline__ u64 granule(u32x2 tag, float v0, float v1) {
  constexpr unsigned M = (1u << TB) - 1u;
  const unsigned a = ((__float_as_uint(v0) + (M + 1u) / 2u) & ~M) | tag[0];
  const unsigned b = ((__float_as_uint(v1) + (M + 1u) / 2u) & ~M) | tag[1];
  return ((u64)b << 32) | a;
}
__device__ __forceinline__ void granule_take(u32x2 tag, u64 g, f32x2& acc, unsigned& bad) {
  const unsigned a = (unsigned)g ^ tag[0], b = (unsigned)(g >> 32) ^ tag[1];
  bad |= a | b;
  acc += f32x2{__uint_as_float(a), __uint_as_float(b)};
}
typedef __amdgpu_buffer_rsrc_t rsrc_t;
constexpr int AUX_SC1_NT = 16 | 2;
__device__ __forceinline__ rsrc_t ws_rsrc(char* ws) { return __builtin_amdgcn_make_buffer_rsrc(ws, 0, 2u << 20, 0x00020000); }
__device__ __forceinline__ void st_dev_nt(rsrc_t rs, unsigned off, u64 v) {
  __builtin_amdgcn_raw_buffer_store_b64(u32x2{(unsigned)v, (unsigned)(v >> 32)}, rs, off, 0, AUX_SC1_NT);
}
__device__ __forceinline__ u64 ld_dev_nt(rsrc_t rs, unsigned off) {
  const u32x2 v = __builtin_amdgcn_raw_buffer_load_b64(rs, off, 0, AUX_SC1_NT);
  return ((u64)v[1] << 32) | v[0];
}
__device__ __forceinline__ void lds_wait(unsigned* w, unsigned n, bool nap) {
#pragma unroll 1
  for (unsigned spins = 0; spins < (SPIN_CAP << 4); ++spins) {
    if (__hip_atomic_load(w, RLX_WG) >= n) break;
    if (nap) __builtin_amdgcn_s_sleep(8);
  }
  asm volatile("" ::: "memory");
}

__global__ void __launch_bounds__(NTHR, 2)
fused_decode_kernel(const unsigned short* __restrict__ x, const char* __restrict__ kc, const char* __restrict__ vc,
                    const char* __restrict__ wq, const char* __restrict__ wo, const char* __restrict__ w1,
                    const char* __restrict__ w3, const char* __restrict__ w2, const unsigned short* __restrict__ gw,
                    unsigned short* __restrict__ out, char* __restrict__ ws) {
  __shared__ __attribute__((aligned(16))) float lds[L_END];
  unsigned* ldsu = (unsigned*)lds;
  const int tid = threadIdx.x, lane = tid & 63, wave = __builtin_amdgcn_readfirstlane(tid >> 6);
  const int r = lane >> 4, c = lane & 15;
  const int bid = blockIdx.x, xg = bid & 7, idx = bid >> 3;
  const int h = xg * 4 + ((idx >> 1) & 3), b = idx >> 3, s = idx & 1;
  const int p = h * 2 + s;
  const int late = ((idx >> 2) ^ (idx >> 4)) & 1;
  const int pslot = late * 32 + (xg & 1) * 16 + (xg >> 1) * 4 + (idx & 3);
  u64 code0;
  asm volatile("s_getpc_b64 %0\n.Lfused_decode_head:" : "=s"(code0));

  const int hw = wave - 4, m = b * 2 + s, cq = m & 3, rp = m >> 2;
  u32x4 wv4[4]; u32x2 xp[4]; u64 ep = 0;
  const unsigned k0 = rp * 256 + (hw & 3) * 64 + 4 * (lane >> 2);
  unsigned wqo[4], xo = k0 * 2;
#pragma unroll
  for (int i = 0; i < 4; ++i) wqo[i] = ((k0 + i) * (NH * DH) + h * DH + 32 * cq + 8 * (lane & 3)) * 2;
  asm volatile("" : "+v"(wqo[0]), "+v"(wqo[1]), "+v"(wqo[2]), "+v"(wqo[3]), "+v"(xo));
  asm volatile("" :: "s"(x), "s"(kc), "s"(vc), "s"(wq), "s"(wo), "s"(w1), "s"(w3), "s"(w2), "s"(gw), "s"(out), "s"(ws));
  const unsigned t0 = (unsigned)__builtin_amdgcn_s_memrealtime();
  SB();
  if (wave >= 4) {
    __builtin_amdgcn_s_setprio(3);
#pragma unroll
    for (int i = 0; i < 4; ++i) wv4[i] = ld_nt(wq + wqo[i]);
#pragma unroll
    for (int bb = 0; bb < 4; ++bb) xp[bb] = *(const u32x2*)((const char*)x + xo + bb * DM * 2);
    ep = __hip_atomic_load((GAS u64*)(ws + WS_EPOCH), RLX_AGENT);
    if (tid == 256) { ldsu[L_CNT + 0] = 0u; ldsu[L_CNT + 1] = 0u; ldsu[L_CNT + 2] = 0u; }
  }
  SB();
  if (idx == 0 && tid == 0) __hip_atomic_store((GAS unsigned*)(ws + WS_T0) + xg, t0, RLX_AGENT);
  BARRIER();

  if (__builtin_expect(wave < 4, 1)) {
    const unsigned chunk = (b * NH + h) * 2 + s;
    const char* kp = kc + (size_t)chunk * 1048576;
    const char* vp = vc + (size_t)chunk * 1048576;
    unsigned xm = 0;
#pragma unroll
    for (int i = 0; i < 8; ++i) xm ^= ((chunk >> i) & 1u) ? (unsigned)((XM >> (8 * i)) & 63u) : 0u;
    auto goff = [&](int g) { return ((g ^ (xm >> 2)) & 15) * 65536; };
    const unsigned phase = ((xg & 1) ? PH_A : 0u) + (late ? PH_B : 0u);
    u32x4 pre[NPRE][16], buf[16];
    Sched sd{t0, (unsigned)((TDELTA) * 25600.0), TAU + (unsigned)((TEXTRA) * 25600.0 / (NEXTRA)), (xm & 3u) << 14,
             (unsigned)(wave * 4096 + lane * 16), NEVER};
    int code_len;
    asm volatile("s_mov_b32 %0, .Lfused_decode_tail-.Lfused_decode_head" : "=s"(code_len));
    if (wave == 3 && idx * 1024 < code_len + 1024 && code_len > 0) {
      const u64 cp = (code0 + min(idx * 1024, code_len)) & ~3ull;
      unsigned cw[16];
      asm volatile("s_load_dword %0, %16, 0x0\n\ts_load_dword %1, %16, 0x40\n\ts_load_dword %2, %16, 0x80\n\ts_load_dword %3, %16, 0xc0\n\t"
                   "s_load_dword %4, %16, 0x100\n\ts_load_dword %5, %16, 0x140\n\ts_load_dword %6, %16, 0x180\n\ts_load_dword %7, %16, 0x1c0\n\t"
                   "s_load_dword %8, %16, 0x200\n\ts_load_dword %9, %16, 0x240\n\ts_load_dword %10, %16, 0x280\n\ts_load_dword %11, %16, 0x2c0\n\t"
                   "s_load_dword %12, %16, 0x300\n\ts_load_dword %13, %16, 0x340\n\ts_load_dword %14, %16, 0x380\n\ts_load_dword %15, %16, 0x3c0\n\t"
                   "s_waitcnt lgkmcnt(0)"
                   : "=&s"(cw[0]), "=&s"(cw[1]), "=&s"(cw[2]), "=&s"(cw[3]), "=&s"(cw[4]), "=&s"(cw[5]), "=&s"(cw[6]), "=&s"(cw[7]),
                     "=&s"(cw[8]), "=&s"(cw[9]), "=&s"(cw[10]), "=&s"(cw[11]), "=&s"(cw[12]), "=&s"(cw[13]), "=&s"(cw[14]), "=&s"(cw[15])
                   : "s"(cp));
    }
    unsigned pf0 = 0, pf1 = 0, pf2 = 0, pf3 = 0;
    {
      const char* pa = wave < 2 ? wo + (size_t)xg * 524288 + idx * 16384 + wave * 8192 + lane * 128
                                : (wave == 2 ? w1 : w3) + (size_t)(16 * idx + (lane >> 2)) * (DF * 2) + xg * 512 + (lane & 3) * 128;
      pace(t0, wave < 2 ? steps(PF_A) : steps(PF_B));
      if (wave < 2 || xg < 7) {
        pf0 = __builtin_nontemporal_load((const unsigned*)pa); SB();
        pf1 = __hip_atomic_load((GAS unsigned*)pa, RLX_AGENT);
      }
      SB();
      if (wave == 1 && lane < 2) pf2 = __builtin_nontemporal_load((const unsigned*)(lane ? vp : kp));
      if (wave == 0 && xg < 7) {
        const char* pb = w2 + (size_t)xg * 262144 + idx * 8192 + lane * 128;
        pace(t0, steps(PF_C)); SB();
        pf2 = __builtin_nontemporal_load((const unsigned*)pb); SB();
        pf3 = __hip_atomic_load((GAS unsigned*)pb, RLX_AGENT);
      }
      SB();
    }
    {
      asm volatile("" :: "s"(xm), "s"(sd.xq), "v"(sd.lo), "s"(kp + goff(0)));
      pace(t0, steps(TREAD));
      u32x8 bc;
      asm volatile("s_load_dwordx8 %0, %1, 0x0\n\ts_waitcnt lgkmcnt(0)" : "=&s"(bc) : "s"(ws + WS_T0) : "memory");
      unsigned emax = 0u;
#pragma unroll
      for (int i = 0; i < 8; ++i) {
        const unsigned e = bc[i] - (t0 - 2000u);
        emax = max(emax, e < 2000u + steps(TREAD) ? e : 0u);
        asm volatile("" : "+s"(emax));
      }
      sd.t0 = t0 + (emax ? emax - 2000u : 0u) + phase;
      if (tid == 0) ldsu[L_CNT + 3] = sd.t0;
    }
    static_for<4 * NPRE + 4>([&](auto tt) {
      constexpr int t = decltype(tt)::v;
      if constexpr (t == (NEXTRA)) sd.tau = TAU;
      if constexpr (t < 4 * NPRE) issue_tick<t & 3, false>(pre[t >> 2], kp + goff(t >> 2), sd);
      else issue_tick<t & 3, false>(buf, kp + goff(NPRE), sd);
    });
    sd.tau = TAU;
    BARRIER();
    asm volatile("" :: "v"(pf0), "v"(pf1), "v"(pf2), "v"(pf3));

    const u32x4 q = *(const u32x4*)(lds + L_Q + 4 * c);
    float* sc = lds + L_SC + wave * 1024 + lane;
    float mx = -INFINITY;
    {
      const float sv = k_group<false, true>(buf, q, kp, kp + goff(NPRE + 1), c, sd, [&](auto qq) {
        constexpr int g = decltype(qq)::v - (3 - NPRE);
        if constexpr (g >= 0 && g < NPRE) {
          const float pv = k_group<false, false>(pre[g], q, kp, kp, c, sd, Nothing{});
          sc[g * 64] = pv;
          mx = fmaxf(mx, pv);
          SB();
        }
      });
      sc[NPRE * 64] = sv;
      mx = fmaxf(mx, sv);
    }
#pragma unroll 1
    for (int g = NPRE + 1; g < 16; ++g) {
      const char* cur = kp + goff(g);
      const char* nxt = ((g < 15) ? kp : vp) + goff(g + 1);
      const float sv = k_group<true, true>(buf, q, cur, nxt, c, sd, Nothing{});
      sc[g * 64] = sv;
      mx = fmaxf(mx, sv);
    }
    float sn = sc[0];
    mx = wave_max(mx);
    if (lane == 0) {
      lds[L_MX + wave] = mx;
      LDS_FENCE();
      __hip_atomic_fetch_add(ldsu + L_CNT + 0, 1u, RLX_WG);
    }
    f32x2 acc[4];
#pragma unroll
    for (int k = 0; k < 4; ++k) acc[k] = f32x2{0.f, 0.f};
#pragma unroll 1
    for (int g = 0; g < 15; ++g) {
      const float pc = __builtin_amdgcn_exp2f((sn - mx) * LOG2E);
      sn = sc[(g + 1) * 64];
      v_group<true>(buf, acc, pc, vp + goff(g), vp + goff(g + 1), sd);
    }
    v_group<false>(buf, acc, __builtin_amdgcn_exp2f((sn - mx) * LOG2E), vp + goff(15), vp, sd);
    *(f32x2*)(lds + L_OP + wave * 128 + 2 * lane) = row_reduce_scatter4(acc);
    BARRIER();
    if (wave == 0 && s == 0) {
      const unsigned tbar = (unsigned)__builtin_amdgcn_s_memrealtime() - sd.t0 + steps(YWAIT);
      const unsigned tlast = ((sd.due - sd.tau) >> 8) - phase + steps(YSCHED);
      const unsigned t_early = max(tlast + YEARLY, pslot < 48 ? tbar : 0u), t_late = max(tlast + PH_A + PH_B, tbar);
      u32x2 tag = tag_of<8>(((u64)ldsu[L_CNT + 5] << 32) | ldsu[L_CNT + 4], lane & 7);
      const rsrc_t rs = ws_rsrc(ws);
      unsigned yin = WS_Y + ((b * 32 + h) * 512 + lane) * 8;
      GAS unsigned short* op = (GAS unsigned short*)out + b * DM + 64 * (h >> 2) + 8 * (lane & 7) + 2 * (h & 3) + (lane >> 5);
      asm volatile("" : "+v"(yin), "+v"(op), "+v"(tag));
      u64 gv[8];
      f32x2 sm; unsigned bad;
      auto take = [&]() {
        sm = f32x2{0.f, 0.f}; bad = 0u;
#pragma unroll
        for (int i = 0; i < 8; ++i) { granule_take(tag, gv[i], sm, bad); asm volatile("" : "+v"(sm), "+v"(bad)); }
      };
      SB(); pace(sd.t0, t_early); SB();
#pragma unroll
      for (int i = 0; i < 6; ++i) gv[i] = ld_dev_nt(rs, yin + i * 512);
      SB(); pace(sd.t0, t_late); SB();
#pragma unroll
      for (int i = 6; i < 8; ++i) gv[i] = ld_dev_nt(rs, yin + i * 512);
      SB();
      take();
      bool ok = __all((bad & 0xffu) == 0u);
      if (__builtin_expect(!ok, 0)) {
#pragma unroll 1
        for (unsigned spins = 0; spins < SPIN_CAP && !ok; ++spins) {
          asm volatile("" ::: "memory");
#pragma unroll
          for (int i = 0; i < 8; ++i) gv[i] = ld_dev_nt(rs, yin + i * 512);
          take();
          ok = __all((bad & 0xffu) == 0u);
        }
        if (!ok) sm[0] = sm[1] = __uint_as_float(0x7fc00000u);
      }
      float sum = rs32(sm[0], sm[1]);
      sum = rs16(sum, sum);
      sum += dpp<ROR8>(sum);
      if ((lane & 31) < 8) *op = (unsigned short)pack_bf16(sum, 0.f);
    }
  } else {
    const unsigned short* xb = x + b * DM;
    {
      float a[32];
#pragma unroll
      for (int k = 0; k < 4; ++k) {
        const unsigned l0 = __builtin_amdgcn_perm(wv4[1][k], wv4[0][k], 0x05040100u), h0 = __builtin_amdgcn_perm(wv4[1][k], wv4[0][k], 0x07060302u);
        const unsigned l1 = __builtin_amdgcn_perm(wv4[3][k], wv4[2][k], 0x05040100u), h1 = __builtin_amdgcn_perm(wv4[3][k], wv4[2][k], 0x07060302u);
#pragma unroll
        for (int bb = 0; bb < 4; ++bb) {
          a[bb * 8 + 2 * k] = dot2(l1, xp[bb][1], dot2(l0, xp[bb][0], 0.f));
          a[bb * 8 + 2 * k + 1] = dot2(h1, xp[bb][1], dot2(h0, xp[bb][0], 0.f));
        }
      }
#pragma unroll
      for (int j = 0; j < 16; ++j) a[j] = rs32(a[j], a[j + 16]);
#pragma unroll
      for (int j = 0; j < 8; ++j) a[j] = rs16(a[j], a[j + 8]);
#pragma unroll
      for (int j = 0; j < 4; ++j) { const float keep = (lane & 8) ? a[j + 4] : a[j], send = (lane & 8) ? a[j] : a[j + 4]; a[j] = keep + dpp<ROR8>(send); }
#pragma unroll
      for (int j = 0; j < 2; ++j) { const float keep = (lane & 4) ? a[j + 2] : a[j], send = (lane & 4) ? a[j] : a[j + 2]; a[j] = keep + dpp<XOR3>(dpp<XOR7>(send)); }
      *(f32x2*)(lds + L_QP + hw * 128 + r * 32 + 8 * (lane & 3) + (c >> 2) * 2) = f32x2{a[0], a[1]};
      asm volatile("s_waitcnt vmcnt(0) lgkmcnt(0)" : "+v"(ep) :: "memory");
      ep += 1;
      if (lane == 0) __hip_atomic_fetch_add(ldsu + L_CNT + 2, 1u, RLX_WG);
      if (hw == 0) {
        lds_wait(ldsu + L_CNT + 2, 4u, false);
        const float* qp = lds + L_QP + 2 * lane;
        const f32x2 qv = (*(const f32x2*)(qp) + *(const f32x2*)(qp + 128)) + (*(const f32x2*)(qp + 256) + *(const f32x2*)(qp + 384));
        const rsrc_t rs = ws_rsrc(ws);
        const u32x2 qtag = tag_of<4>(ep, c);
        st_dev_nt(rs, WS_Q + (((h * 4 + r) * 2 + rp) * 64 + 16 * cq + c) * 8, granule<4>(qtag, qv[0], qv[1]));
        pace_lazy(t0, steps(QWAIT));
        f32x2 qs = {0.f, 0.f}; bool ok = false;
#pragma unroll 1
        for (unsigned spins = 0; spins < SPIN_CAP; ++spins) {
          asm volatile("" ::: "memory");
          const u64 g0 = ld_dev_nt(rs, WS_Q + ((h * 4 + b) * 128 + lane) * 8), g1 = ld_dev_nt(rs, WS_Q + ((h * 4 + b) * 128 + 64 + lane) * 8);
          unsigned bad = 0u;
          qs = f32x2{0.f, 0.f};
          granule_take(qtag, g0, qs, bad); granule_take(qtag, g1, qs, bad);
          ok = __all((bad & 15u) == 0u);
          if (ok) break;
        }
        if (!ok) qs[0] = __uint_as_float(0x7fc00000u);
        ldsu[L_Q + lane] = pack_bf16(qs[0], qs[1]);
        if (lane == 0) { ldsu[L_CNT + 4] = (unsigned)ep; ldsu[L_CNT + 5] = (unsigned)(ep >> 32); }
      }
    }
    __builtin_amdgcn_s_setprio(0);
    BARRIER();
    float g, og;
    {
      const u32x4 xv = *(const u32x4*)(xb + 8 * lane), gv = *(const u32x4*)(gw + 8 * lane);
      float z = 0.f;
#pragma unroll
      for (int k = 0; k < 4; ++k) z += rbf(bf_lo(xv[k]) * bf_lo(gv[k])) + rbf(bf_hi(xv[k]) * bf_hi(gv[k]));
      z = rbf(wave_sum(z));
      g = rbf(1.f / (1.f + __expf(-z)));
      og = rbf(1.f - g);
    }
    const unsigned tb = __builtin_amdgcn_readfirstlane(ldsu[L_CNT + 3]) + hw * (unsigned)(HSTAG * TAU / 256.0);
    constexpr double H0 = 4 * NPRE + 6.5;
    auto hdue = [](double ticks) constexpr { return (unsigned)(ticks * TAU / 256.0 + ((TDELTA) + (TEXTRA)) * 100.0); };
    float yacc[8];
#pragma unroll
    for (int k = 0; k < 8; ++k) yacc[k] = 0.f;
    if (p < DF / 32) {
      const int r16 = lane >> 2, c4 = lane & 3;
      const size_t fo = ((size_t)(32 * r16 + hw) * DF + 32 * p + 8 * c4) * 2;
      u32x4 a[8], u[8], dv[8]; float xk[8];
#pragma unroll
      for (int i = 0; i < 8; ++i) xk[i] = bf_lo(xb[32 * r16 + 4 * i + hw]);
      static_for<8>([&](auto ii) {
        constexpr int i = decltype(ii)::v;
        SB(); pace_lazy(tb, hdue(H0 + (3 * i + 0) * HSP_F)); SB(); a[i] = ld_nt(w1 + fo + (size_t)i * 4 * DF * 2);
        SB(); pace_lazy(tb, hdue(H0 + (3 * i + 1) * HSP_F)); SB(); u[i] = ld_nt(w3 + fo + (size_t)i * 4 * DF * 2);
        SB(); pace_lazy(tb, hdue(H0 + (3 * i + 2) * HSP_F)); SB(); dv[i] = ld_nt(w2 + ((size_t)(32 * p + 4 * (i ^ (p & 7)) + r) * DM + 128 * hw + 8 * c) * 2);
        SB();
      });
      float ga[8], ua[8];
#pragma unroll
      for (int k = 0; k < 8; ++k) { ga[k] = 0.f; ua[k] = 0.f; }
#pragma unroll
      for (int i = 0; i < 8; ++i) { fma8(ga, xk[i], a[i]); fma8(ua, xk[i], u[i]); }
#pragma unroll
      for (int k = 0; k < 8; ++k) {
#pragma unroll
        for (int w = 4; w < 64; w <<= 1) { ga[k] += __shfl_xor(ga[k], w); ua[k] += __shfl_xor(ua[k], w); }
      }
      if (lane < 4) {
#pragma unroll
        for (int k = 0; k < 8; ++k) { lds[L_FP + hw * 64 + 8 * lane + k] = ga[k]; lds[L_FP + hw * 64 + 32 + 8 * lane + k] = ua[k]; }
      }
      LDS_FENCE();
      if (lane == 0) __hip_atomic_fetch_add(ldsu + L_CNT + 1, 1u, RLX_WG);
      lds_wait(ldsu + L_CNT + 1, 4u, true);
      const int f = 4 * ((c ^ p) & 7) + r;
      const float gt = rbf((lds[L_FP + f] + lds[L_FP + 64 + f]) + (lds[L_FP + 128 + f] + lds[L_FP + 192 + f]));
      const float up = rbf((lds[L_FP + 32 + f] + lds[L_FP + 96 + f]) + (lds[L_FP + 160 + f] + lds[L_FP + 224 + f]));
      const float hv = og * rbf(rbf(gt / (1.f + __expf(-gt))) * up);
      static_for<8>([&](auto ii) { constexpr int i = decltype(ii)::v; fma8(yacc, dpp<BCAST + i>(hv), dv[i]); });
    }
    GAS u64* ml = (GAS u64*)(ws + WS_ML);
    const unsigned tag = (unsigned)ep;
    float mw = 0.f, lw = 0.f;
    if (hw == 0) {
      lds_wait(ldsu + L_CNT + 0, 4u, true);
      const f32x4 m4 = *(const f32x4*)(lds + L_MX);
      mw = fmaxf(fmaxf(m4[0], m4[1]), fmaxf(m4[2], m4[3]));
#pragma unroll 4
      for (int i = 0; i < 16; ++i) {
        const f32x4 sv = *(const f32x4*)(lds + L_SC + i * 256 + 4 * lane);
#pragma unroll
        for (int k = 0; k < 4; ++k) lw += __builtin_amdgcn_exp2f((sv[k] - mw) * LOG2E);
      }
      lw = wave_sum(lw);
      if (lane < 2) __hip_atomic_store(ml + (b * 64 + p) * 16 + lane, ((u64)tag << 32) | __float_as_uint(lane ? lw : mw), RLX_AGENT);
    }
    u32x4 bf[8][4];
    {
      u32x4 wv[32];
      const char* wp = wo + ((size_t)(h * DH + r) * DM + 128 * hw + 8 * c) * 2;
      static_for<32>([&](auto ii) {
        constexpr int i = decltype(ii)::v;
        SB(); pace_lazy(tb, hdue(H0 + 24 * HSP_F + i * HSP_O)); SB(); wv[i] = ld_nt(wp + (size_t)(i ^ (h & 7)) * 4 * DM * 2); SB();
      });
#pragma unroll
      for (int ks = 0; ks < 4; ++ks) {
#pragma unroll
        for (int k = 0; k < 4; ++k) {
#pragma unroll
          for (int qd = 0; qd < 4; ++qd) {
            const unsigned e0 = wv[8 * ks + 2 * qd][k], e1 = wv[8 * ks + 2 * qd + 1][k];
            bf[2 * k][ks][qd] = __builtin_amdgcn_perm(e1, e0, 0x05040100u);
            bf[2 * k + 1][ks][qd] = __builtin_amdgcn_perm(e1, e0, 0x07060302u);
          }
        }
#pragma unroll
        for (int t = 0; t < 8; ++t) asm volatile("" : "+v"(bf[t][ks]));
      }
    }
    f32x2 yf = row_reduce_scatter(yacc);
    asm volatile("" : "+v"(yf));
    if (hw == 0) {
      float mo = __uint_as_float(0x7fc00000u), lo = mo;
#pragma unroll 1
      for (unsigned spins = 0; spins < SPIN_CAP; ++spins) {
        const u64 o0 = __hip_atomic_load(ml + (b * 64 + (p ^ 1)) * 16, RLX_AGENT), o1 = __hip_atomic_load(ml + (b * 64 + (p ^ 1)) * 16 + 1, RLX_AGENT);
        if ((unsigned)(o0 >> 32) == tag && (unsigned)(o1 >> 32) == tag) { mo = __uint_as_float((unsigned)o0); lo = __uint_as_float((unsigned)o1); break; }
        __builtin_amdgcn_s_sleep(8);
      }
      const float mt = fmaxf(mw, mo);
      const float lt = lw * __builtin_amdgcn_exp2f((mw - mt) * LOG2E) + lo * __builtin_amdgcn_exp2f((mo - mt) * LOG2E);
      if (lane < 4) lds[L_CW + lane] = g * __builtin_amdgcn_exp2f((lds[L_MX + lane] - mt) * LOG2E) / lt;
    }
    if (bid == 0 && hw == 1) {
#pragma unroll 1
      for (unsigned spins = 0; spins < SPIN_CAP; ++spins) {
        const u64 o = __hip_atomic_load(ml + (lane & 31) * 32, RLX_AGENT);
        if (__all((unsigned)(o >> 32) == tag)) break;
        __builtin_amdgcn_s_sleep(8);
      }
      if (lane == 0) __hip_atomic_store((GAS u64*)(ws + WS_EPOCH), ep, RLX_AGENT);
    }
    GAS u64* yo = (GAS u64*)(ws + WS_Y) + ((size_t)(b * 32 + hw * 8 + (c >> 3) * 4 + r) * 64 + pslot) * 8 + (c & 7);
    u32x2 ytag = tag_of<8>(ep, c & 7);
    asm volatile("" : "+v"(yo), "+v"(ytag));
    const bool rodd = (lane & 16) != 0, rhi = (lane & 32) != 0;
    const float* op_src = lds + L_OP + 2 * (lane ^ ((h & 7) >> 1));
    __builtin_amdgcn_s_setprio(3);
    BARRIER();
    {
      const f32x4 cw = *(const f32x4*)(lds + L_CW);
      f32x2 o = cw[0] * *(const f32x2*)(op_src);
#pragma unroll
      for (int w = 1; w < 4; ++w) o += cw[w] * *(const f32x2*)(op_src + w * 128);
      const unsigned opk = pack_bf16(o[0], o[1]);
      const unsigned op = __builtin_amdgcn_alignbit(opk, opk, (h & 1) * 16);
      f32x4 d[8];
#pragma unroll
      for (int t = 0; t < 8; ++t) d[t] = f32x4{0.f, 0.f, 0.f, 0.f};
      u32x4 af[4];
      af[0] = u32x4{dppu<BCAST + 0>(op), dppu<BCAST + 1>(op), dppu<BCAST + 2>(op), dppu<BCAST + 3>(op)};
      SB();
      static_for<4>([&](auto kk) {
        constexpr int ks = decltype(kk)::v;
        static_for<8>([&](auto tt) {
          constexpr int t = decltype(tt)::v;
          d[t] = __builtin_amdgcn_mfma_f32_16x16x32_bf16(__builtin_bit_cast(bf16x8, af[ks]), __builtin_bit_cast(bf16x8, bf[t][ks]), d[t], 0, 0, 0);
          if constexpr (ks < 3 && t < 4) af[ks + 1][t] = dppu<BCAST + 4 * ks + 4 + t>(op);
          SB();
        });
      });
      const float y0 = yf[0] + (rodd ? (rhi ? d[6][0] : d[2][0]) : (rhi ? d[4][0] : d[0][0]));
      const float y1 = yf[1] + (rodd ? (rhi ? d[7][0] : d[3][0]) : (rhi ? d[5][0] : d[1][0]));
      __hip_atomic_store(yo, granule<8>(ytag, y0, y1), RLX_AGENT);
      asm volatile(".Lfused_decode_tail:");
    }
  }
}

extern "C" void kernel_launch(void* const* d_in, const int* in_sizes, int n_in, void* d_out, int out_size, void* d_ws,
                              size_t ws_size, hipStream_t stream) {
  (void)in_sizes; (void)n_in; (void)out_size; (void)ws_size;
  fused_decode_kernel<<<NWG, NTHR, 0, stream>>>((const unsigned short*)d_in[0], (const char*)d_in[1], (const char*)d_in[2],
                                                (const char*)d_in[3], (const char*)d_in[4], (const char*)d_in[6],
                                                (const char*)d_in[7], (const char*)d_in[8], (const unsigned short*)d_in[9],
                                                (unsigned short*)d_out, (char*)d_ws);
}
```
